```python
import torch

def _softmax(x, axis=-1):
    return torch.softmax(x, dim=axis)
def _gelu(x):
    return torch.nn.functional.gelu(x, approximate="tanh")
def _silu(x):
    return torch.nn.functional.silu(x)
def _sigmoid(x):
    return torch.sigmoid(x)
def _layernorm(x, axis=-1, eps=1e-5):
    m = x.mean(dim=axis, keepdim=True)
    v = x.var(dim=axis, keepdim=True, unbiased=False)
    return (x - m) / torch.sqrt(v + eps)
def _scatter_add(tgt, idx, src, axis=0):
    return tgt.clone().index_add_(axis, idx, src)
def _segment_sum(src, seg, n):
    out = torch.zeros((n,) + src.shape[1:], dtype=src.dtype)
    return out.index_add_(0, seg, src)
def _pad(x, pw):
    flat = tuple(v for pair in reversed(pw) for v in pair)
    return torch.nn.functional.pad(x, flat)

kernel_name = 'recurrent_scan__gqa_block_cached__glu_gated_geglu__decode'

def setup_inputs():

    _w = (lambda a: (lambda t: t.to(torch.bfloat16) if t.is_floating_point() else t)(torch.as_tensor(a)))
    def _A_setup():
        B, Sq, D = 1, 1, 512
        Hq, Hkv, Sk, Dh = 32, 8, 32768, 128
        assert Hq % Hkv == 0, (Hq, Hkv)
        return {
            "x": _w((lambda *s: torch.randn(*s, dtype=torch.bfloat16) * 0.4)(B, Sq, D)),
            "k_cache": _w((lambda *s: torch.randn(*s, dtype=torch.bfloat16) * 1.6)(B, Hkv, Sk, Dh)),
            "v_cache": _w((lambda *s: torch.randn(*s, dtype=torch.bfloat16) * 1.6)(B, Hkv, Sk, Dh)),
            "wq": _w((lambda *s: torch.randn(*s, dtype=torch.bfloat16) * 1.6)(D, Hq * Dh) * (D ** -0.5)),
            "wo": _w((lambda *s: torch.randn(*s, dtype=torch.bfloat16) * 1.6)(Hq * Dh, D) * ((Hq * Dh) ** -0.5)),
        }
    _A_in = _A_setup()

    def _B_setup():
        B, S, Dm, Df = 1, 1, 512, 1792
        return {
            "x": _w((lambda *s: torch.randn(*s, dtype=torch.bfloat16) * 0.4)(B, S, Dm)),
            "w_gate": _w((lambda *s: torch.randn(*s, dtype=torch.bfloat16) * 1.6)(Dm, Df) * (Dm ** -0.5)),
            "w_up": _w((lambda *s: torch.randn(*s, dtype=torch.bfloat16) * 1.6)(Dm, Df) * (Dm ** -0.5)),
            "w_down": _w((lambda *s: torch.randn(*s, dtype=torch.bfloat16) * 1.6)(Df, Dm) * (Df ** -0.5)),
        }
    _B_in = _B_setup()

    return {
        "A_x": _A_in["x"],
        "A_k_cache": _A_in["k_cache"],
        "A_v_cache": _A_in["v_cache"],
        "A_wq": _A_in["wq"],
        "A_wo": _A_in["wo"],
        "B_x": _B_in["x"],
        "B_w_gate": _B_in["w_gate"],
        "B_w_up": _B_in["w_up"],
        "B_w_down": _B_in["w_down"],
    }

def reference(A_x, A_k_cache, A_v_cache, A_wq, A_wo, B_x, B_w_gate, B_w_up, B_w_down):
    def _A_ref(x, k_cache, v_cache, wq, wo):
        B, Sq, D = x.shape
        Hq, Dh = 32, 128
        Hkv = k_cache.shape[1]
        rep = Hq // Hkv
        q = (lambda x, *d: x.permute(*d))((x @ wq).reshape(B, Sq, Hq, Dh), 0, 2, 1, 3)
        k = (lambda x, n, axis: torch.repeat_interleave(x, n, dim=axis))(k_cache, rep, axis=1)
        v = (lambda x, n, axis: torch.repeat_interleave(x, n, dim=axis))(v_cache, rep, axis=1)
        scores = torch.einsum("bhqd,bhkd->bhqk", q, k) * (Dh ** -0.5)
        p = _softmax(scores, axis=-1)
        o = torch.einsum("bhqk,bhkd->bhqd", p, v)
        return (lambda x, *d: x.permute(*d))(o, 0, 2, 1, 3).reshape(B, Sq, Hq * Dh) @ wo

    def _B_ref(x, w_gate, w_up, w_down):
        g = torch.einsum("bsd,df->bsf", x, w_gate)
        u = torch.einsum("bsd,df->bsf", x, w_up)
        h = _gelu(g) * u
        return torch.einsum("bsf,fd->bsd", h, w_down)

    x = A_x
    xn = _layernorm(x, axis=-1)
    s = _A_ref(xn, A_k_cache, A_v_cache, A_wq, A_wo)
    g = _silu(_B_ref(xn, B_w_gate, B_w_up, B_w_down))
    return x + s * g
```

```cpp
#include <hip/hip_runtime.h>
#include <hip/hip_bf16.h>
#include <stdint.h>

using u32x4 = __attribute__((ext_vector_type(4))) unsigned;
using u32x2 = __attribute__((ext_vector_type(2))) unsigned;
using f32x4 = __attribute__((ext_vector_type(4))) float;
using bf16x8 = __attribute__((ext_vector_type(8))) __bf16;
using bf16x2 = __attribute__((ext_vector_type(2))) __bf16;
using s16x4 = __attribute__((ext_vector_type(4))) short;
using f32x2 = __attribute__((ext_vector_type(2))) float;

__device__ __forceinline__ float bflo(unsigned v) { return __uint_as_float(v << 16); }
__device__ __forceinline__ float bfhi(unsigned v) { return __uint_as_float(v & 0xffff0000u); }
__device__ __forceinline__ unsigned pk_bf16(float a, float b) {
  f32x2 v = {a, b};
  bf16x2 r = __builtin_convertvector(v, bf16x2);
  return __builtin_bit_cast(unsigned, r);
}
__device__ __forceinline__ float rbf(float a) { return bflo(pk_bf16(a, a)); }
__device__ __forceinline__ float wave_sum(float v) {
#pragma unroll
  for (int o = 32; o > 0; o >>= 1) v += __shfl_xor(v, o);
  return v;
}

constexpr size_t WS_Q = 0;
constexpr size_t WS_DP = 65536;
constexpr size_t WS_OP = WS_DP + 256 * 512 * 4;
constexpr size_t WS_ML = WS_OP + 256 * 512 * 4;
constexpr size_t WS_YP = WS_ML + 256 * 8 * 4;

__global__ void __launch_bounds__(512) k_proj(const unsigned short* __restrict__ x, const char* __restrict__ wq,
                                              const char* __restrict__ wg, const char* __restrict__ wu,
                                              const char* __restrict__ wd, char* __restrict__ ws) {
  const int b = blockIdx.x, t = threadIdx.x, lane = t & 63, wave = t >> 6;
  __shared__ float red[8][32];
  __shared__ float hs[8];
  const int cb = (b & 7) * 32 + (b >> 3);
  const u32x4 w0 = *(const u32x4*)(wq + (size_t)t * 8192 + cb * 32);
  const u32x4 w1 = *(const u32x4*)(wq + (size_t)t * 8192 + cb * 32 + 16);
  const bool mlp = (b >> 3) < 28;
  const int fb = (b & 7) * 28 + (b >> 3);
  u32x4 g4 = {0, 0, 0, 0}, u4 = {0, 0, 0, 0};
  if (mlp) {
    g4 = *(const u32x4*)(wg + (size_t)t * 3584 + fb * 16);
    u4 = *(const u32x4*)(wu + (size_t)t * 3584 + fb * 16);
  }
  const u32x4 xv = *(const u32x4*)((const char*)x + lane * 16);
  float s = 0;
#pragma unroll
  for (int i = 0; i < 4; ++i) s += bflo(xv[i]) + bfhi(xv[i]);
  s = wave_sum(s);
  const float mean = s * (1.0f / 512.0f);
  float ss = 0;
#pragma unroll
  for (int i = 0; i < 4; ++i) { float a = bflo(xv[i]) - mean, c = bfhi(xv[i]) - mean; ss += a * a + c * c; }
  ss = wave_sum(ss);
  const float var = rbf(ss * (1.0f / 512.0f));
  const float sd = rbf(sqrtf(rbf(var + 1e-5f)));
  const float xt = bflo(x[t]);
  const float xn = rbf(rbf(xt - rbf(mean)) / sd);
  float a[32];
#pragma unroll
  for (int i = 0; i < 4; ++i) {
    a[2 * i] = xn * bflo(w0[i]); a[2 * i + 1] = xn * bfhi(w0[i]);
    a[8 + 2 * i] = xn * bflo(w1[i]); a[8 + 2 * i + 1] = xn * bfhi(w1[i]);
    a[16 + 2 * i] = xn * bflo(g4[i]); a[16 + 2 * i + 1] = xn * bfhi(g4[i]);
    a[24 + 2 * i] = xn * bflo(u4[i]); a[24 + 2 * i + 1] = xn * bfhi(u4[i]);
  }
#pragma unroll
  for (int st = 0; st < 5; ++st) {
    const int n = 16 >> st;
    const int m = 32 >> st;
    const bool up = (lane & m) != 0;
#pragma unroll
    for (int i = 0; i < n; ++i) {
      float keep = up ? a[i + n] : a[i];
      float send = up ? a[i] : a[i + n];
      a[i] = keep + __shfl_xor(send, m);
    }
  }
  a[0] += __shfl_xor(a[0], 1);
  if ((lane & 1) == 0) red[wave][lane >> 1] = a[0];
  __syncthreads();
  if (t < 32) {
    float v = 0;
#pragma unroll
    for (int w = 0; w < 8; ++w) v += red[w][t];
    v = rbf(v);
    if (t < 16) ((unsigned short*)(ws + WS_Q))[cb * 16 + t] = (unsigned short)(__float_as_uint(v) >> 16);
    float u = __shfl(v, (t & 7) + 24);
    if (t >= 16 && t < 24) {
      float inner = 0.7978845608028654f * (v + 0.044715f * v * v * v);
      float ge = rbf(0.5f * v * (1.0f + tanhf(inner)));
      hs[t - 16] = rbf(ge * u);
    }
  }
  __syncthreads();
  if (mlp) {
    float d = 0;
#pragma unroll
    for (int r = 0; r < 8; ++r) d += hs[r] * bflo(((const unsigned short*)wd)[(size_t)(fb * 8 + r) * 512 + t]);
    ((float*)(ws + WS_DP))[fb * 512 + t] = d;
  }
}

__global__ void __launch_bounds__(512, 2) k_attn(const char* __restrict__ K, const char* __restrict__ V, char* __restrict__ ws) {
  const int b = blockIdx.x, tid = threadIdx.x, lane = tid & 63, wave = __builtin_amdgcn_readfirstlane(tid >> 6);
  const int h = b & 7, j = b >> 3;
  const int g = lane >> 4, i = lane & 15;
  constexpr int TILES = 8, WIN = 4;
  const size_t base = ((size_t)h * 32768 + j * 1024 + wave * 128) * 256;
  const char* kp = K + base + (4 * (i & 3) + (i >> 2)) * 256 + g * 16;
  const char* vp = V + base + lane * 16;
  u32x4 kf[TILES][4], vf[TILES][4];
#pragma unroll
  for (int t = 0; t < WIN; ++t) {
#pragma unroll
    for (int u = 0; u < 4; ++u) kf[t][u] = __builtin_nontemporal_load((const u32x4*)(kp + t * 4096 + u * 64));
#pragma unroll
    for (int u = 0; u < 4; ++u) vf[t][u] = __builtin_nontemporal_load((const u32x4*)(vp + t * 4096 + u * 1024));
  }
  u32x4 qf[4];
#pragma unroll
  for (int u = 0; u < 4; ++u) qf[u] = *(const u32x4*)(ws + WS_Q + ((h * 4 + (i & 3)) * 128 + u * 32 + g * 8) * 2);
  const int e = i >> 2;
  const unsigned mask0 = e == 0 ? 0xffffu : e == 1 ? 0xffff0000u : 0u;
  const unsigned mask1 = e == 2 ? 0xffffu : e == 3 ? 0xffff0000u : 0u;
  f32x4 olo = {0, 0, 0, 0}, ohi = {0, 0, 0, 0};
  float m = -INFINITY, l = 0;
  constexpr float SCALE = 0.08838834764831845f, LOG2E = 1.4426950408889634f;
  __builtin_amdgcn_sched_barrier(0);
#pragma unroll
  for (int t = 0; t < TILES; ++t) {
    f32x4 s = {0, 0, 0, 0};
#pragma unroll
    for (int u = 0; u < 4; ++u)
      s = __builtin_amdgcn_mfma_f32_16x16x32_bf16(__builtin_bit_cast(bf16x8, kf[t][u]), __builtin_bit_cast(bf16x8, qf[u]), s, 0, 0, 0);
    float z[4];
    {
      unsigned p01 = pk_bf16(s[0], s[1]), p23 = pk_bf16(s[2], s[3]);
      p01 = pk_bf16(bflo(p01) * SCALE, bfhi(p01) * SCALE);
      p23 = pk_bf16(bflo(p23) * SCALE, bfhi(p23) * SCALE);
      z[0] = bflo(p01); z[1] = bfhi(p01); z[2] = bflo(p23); z[3] = bfhi(p23);
    }
    float mx = fmaxf(fmaxf(z[0], z[1]), fmaxf(z[2], z[3]));
    mx = fmaxf(mx, __shfl_xor(mx, 16));
    mx = fmaxf(mx, __shfl_xor(mx, 32));
    const float mn = fmaxf(m, mx);
    const float sc = __builtin_amdgcn_exp2f((m - mn) * LOG2E);
    m = mn;
    const float mb = mn * LOG2E;
    olo *= sc; ohi *= sc; l *= sc;
#pragma unroll
    for (int u = 0; u < 4; ++u) {
      const float p = __builtin_amdgcn_exp2f(z[u] * LOG2E - mb);
      const unsigned pp = pk_bf16(p, p);
      l += bflo(pp);
      u32x2 bb = {pp & mask0, pp & mask1};
      u32x2 vl = {vf[t][u][0], vf[t][u][1]}, vh = {vf[t][u][2], vf[t][u][3]};
      olo = __builtin_amdgcn_mfma_f32_16x16x16bf16_1k(__builtin_bit_cast(s16x4, vl), __builtin_bit_cast(s16x4, bb), olo, 0, 0, 0);
      ohi = __builtin_amdgcn_mfma_f32_16x16x16bf16_1k(__builtin_bit_cast(s16x4, vh), __builtin_bit_cast(s16x4, bb), ohi, 0, 0, 0);
    }
    __builtin_amdgcn_sched_barrier(0);
    if (t + WIN < TILES) {
#pragma unroll
      for (int u = 0; u < 4; ++u) kf[t + WIN][u] = __builtin_nontemporal_load((const u32x4*)(kp + (t + WIN) * 4096 + u * 64));
#pragma unroll
      for (int u = 0; u < 4; ++u) vf[t + WIN][u] = __builtin_nontemporal_load((const u32x4*)(vp + (t + WIN) * 4096 + u * 1024));
    }
    __builtin_amdgcn_sched_barrier(0);
  }
  l += __shfl_xor(l, 16);
  l += __shfl_xor(l, 32);
  __shared__ float so[8][4][128];
  __shared__ float sm[8][4], sl[8][4];
  __shared__ char padlds[64 * 1024];
  if (ws == nullptr) padlds[tid * 97] = 1;
#pragma unroll
  for (int r = 0; r < 4; ++r) {
    so[wave][i & 3][8 * (4 * g + r) + e] = olo[r];
    so[wave][i & 3][8 * (4 * g + r) + 4 + e] = ohi[r];
  }
  if (lane < 4) { sm[wave][lane] = m; sl[wave][lane] = l; }
  __syncthreads();
  {
    const int hh = tid >> 7, d = tid & 127;
    float M = sm[0][hh];
#pragma unroll
    for (int w = 1; w < 8; ++w) M = fmaxf(M, sm[w][hh]);
    float o = 0, L = 0;
#pragma unroll
    for (int w = 0; w < 8; ++w) { float f = __builtin_amdgcn_exp2f((sm[w][hh] - M) * LOG2E); o += f * so[w][hh][d]; L += f * sl[w][hh]; }
    ((float*)(ws + WS_OP))[b * 512 + tid] = o;
    if (d == 0) { ((float*)(ws + WS_ML))[b * 8 + hh] = M; ((float*)(ws + WS_ML))[b * 8 + 4 + hh] = L; }
    if (ws == nullptr) ((float*)(ws))[tid] = padlds[tid * 31];
  }
}

__global__ void __launch_bounds__(512) k_comb(const char* __restrict__ wo, char* __restrict__ ws) {
  const int b = blockIdx.x, tid = threadIdx.x;
  const int h = b & 7, j = b >> 3;
  constexpr float LOG2E = 1.4426950408889634f;
  __shared__ float part[32][16];
  __shared__ float ov[16];
  const int f0 = h * 512 + j * 16;
  unsigned short w[16];
#pragma unroll
  for (int r = 0; r < 16; ++r) w[r] = ((const unsigned short*)wo)[(size_t)(f0 + r) * 512 + tid];
  const int p = tid >> 4, dd = tid & 15, hh = j >> 3;
  const float* ml = (const float*)(ws + WS_ML);
  float M = -INFINITY;
  for (int s = 0; s < 32; ++s) M = fmaxf(M, ml[(s * 8 + h) * 8 + hh]);
  float L = 0;
  for (int s = 0; s < 32; ++s) L += __builtin_amdgcn_exp2f((ml[(s * 8 + h) * 8 + hh] - M) * LOG2E) * ml[(s * 8 + h) * 8 + 4 + hh];
  const float f = __builtin_amdgcn_exp2f((ml[(p * 8 + h) * 8 + hh] - M) * LOG2E);
  part[p][dd] = f * ((const float*)(ws + WS_OP))[(p * 8 + h) * 512 + j * 16 + dd];
  __syncthreads();
  if (tid < 16) {
    float o = 0;
    for (int s = 0; s < 32; ++s) o += part[s][tid];
    ov[tid] = rbf(o / L);
  }
  __syncthreads();
  float y = 0;
#pragma unroll
  for (int r = 0; r < 16; ++r) y += ov[r] * bflo(w[r]);
  ((float*)(ws + WS_YP))[b * 512 + tid] = y;
}

__global__ void __launch_bounds__(512) k_final(const unsigned short* __restrict__ x, const char* __restrict__ ws, unsigned short* __restrict__ out) {
  const int b = blockIdx.x, tid = threadIdx.x, c = tid & 15, r = tid >> 4;
  __shared__ float sy[32][16], sd[32][16];
  float y = 0, d = 0;
#pragma unroll
  for (int k = 0; k < 8; ++k) y += ((const float*)(ws + WS_YP))[(k * 32 + r) * 512 + b * 16 + c];
#pragma unroll
  for (int k = 0; k < 7; ++k) d += ((const float*)(ws + WS_DP))[(k * 32 + r) * 512 + b * 16 + c];
  sy[r][c] = y; sd[r][c] = d;
  __syncthreads();
  if (tid < 16) {
    float Y = 0, D = 0;
    for (int k = 0; k < 32; ++k) { Y += sy[k][tid]; D += sd[k][tid]; }
    Y = rbf(Y); D = rbf(D);
    float gt = rbf(D / (1.0f + __expf(-D)));
    float res = rbf(bflo(x[b * 16 + tid]) + rbf(Y * gt));
    out[b * 16 + tid] = (unsigned short)(__float_as_uint(res) >> 16);
  }
}

extern "C" void kernel_launch(void* const* d_in, const int* in_sizes, int n_in, void* d_out, int out_size, void* d_ws,
                              size_t ws_size, hipStream_t stream) {
  const unsigned short* x = (const unsigned short*)d_in[0];
  char* ws = (char*)d_ws;
  k_proj<<<256, 512, 0, stream>>>(x, (const char*)d_in[3], (const char*)d_in[6], (const char*)d_in[7], (const char*)d_in[8], ws);
  k_attn<<<256, 512, 0, stream>>>((const char*)d_in[1], (const char*)d_in[2], ws);
  k_comb<<<256, 512, 0, stream>>>((const char*)d_in[4], ws);
  k_final<<<32, 512, 0, stream>>>(x, ws, (unsigned short*)d_out);
}
```

```cpp
#include <hip/hip_runtime.h>
#include <hip/hip_bf16.h>
#include <stdint.h>

using u32x4 = __attribute__((ext_vector_type(4))) unsigned;
using u32x2 = __attribute__((ext_vector_type(2))) unsigned;
using f32x4 = __attribute__((ext_vector_type(4))) float;
using f32x2 = __attribute__((ext_vector_type(2))) float;
using bf16x8 = __attribute__((ext_vector_type(8))) __bf16;
using bf16x2 = __attribute__((ext_vector_type(2))) __bf16;
using s16x4 = __attribute__((ext_vector_type(4))) short;
typedef unsigned long long u64;
typedef __attribute__((address_space(1))) u64 gu64;

#define DEV __device__ __forceinline__
DEV float bflo(unsigned v) { return __uint_as_float(v << 16); }
DEV float bfhi(unsigned v) { return __uint_as_float(v & 0xffff0000u); }
DEV unsigned pk_bf16(float a, float b) {
  f32x2 v = {a, b};
  return __builtin_bit_cast(unsigned, __builtin_convertvector(v, bf16x2));
}
DEV float rbf(float a) { return bflo(pk_bf16(a, a)); }
template <int C> DEV float dpp(float v) {
  return __int_as_float(__builtin_amdgcn_update_dpp(0, __float_as_int(v), C, 0xf, 0xf, true));
}
DEV float row_sum(float v) {
  v += dpp<0xB1>(v); v += dpp<0x4E>(v); v += dpp<0x141>(v); v += dpp<0x140>(v); return v;
}
DEV float row_max(float v) {
  v = fmaxf(v, dpp<0xB1>(v)); v = fmaxf(v, dpp<0x4E>(v)); v = fmaxf(v, dpp<0x141>(v)); v = fmaxf(v, dpp<0x140>(v)); return v;
}
DEV void swap16(float& a, float& b) {
  auto r = __builtin_amdgcn_permlane16_swap(__float_as_uint(a), __float_as_uint(b), false, false);
  a = __uint_as_float(r[0]); b = __uint_as_float(r[1]);
}
DEV void swap32(float& a, float& b) {
  auto r = __builtin_amdgcn_permlane32_swap(__float_as_uint(a), __float_as_uint(b), false, false);
  a = __uint_as_float(r[0]); b = __uint_as_float(r[1]);
}
DEV float xsum16(float v) { float a = v, b = v; swap16(a, b); return a + b; }
DEV float xsum32(float v) { float a = v, b = v; swap32(a, b); return a + b; }
DEV float xmax16(float v) { float a = v, b = v; swap16(a, b); return fmaxf(a, b); }
DEV float xmax32(float v) { float a = v, b = v; swap32(a, b); return fmaxf(a, b); }
DEV float wave_sum(float v) { return xsum32(xsum16(row_sum(v))); }
DEV void wg_barrier() { asm volatile("s_waitcnt lgkmcnt(0)\n\ts_barrier" ::: "memory"); }

DEV void reduce16(float (&a)[16]) {
#pragma unroll
  for (int i = 0; i < 8; ++i) { swap32(a[i], a[i + 8]); a[i] += a[i + 8]; }
#pragma unroll
  for (int i = 0; i < 4; ++i) { swap16(a[i], a[i + 4]); a[i] += a[i + 4]; }
#pragma unroll
  for (int i = 0; i < 4; ++i) a[i] = row_sum(a[i]);
}

DEV void put(char* base, unsigned idx, float v, unsigned tlo, unsigned thi) {
  const u64 w = ((u64)((__float_as_uint(v) & 0xffffff00u) | thi) << 32) | tlo;
  __hip_atomic_store((gu64*)(base) + idx, w, __ATOMIC_RELAXED, __HIP_MEMORY_SCOPE_AGENT);
}
DEV u64 get(const char* base, unsigned idx) {
  return __hip_atomic_load((gu64*)(base) + idx, __ATOMIC_RELAXED, __HIP_MEMORY_SCOPE_AGENT);
}
DEV bool fresh(u64 w, unsigned tlo, unsigned thi) { return (unsigned)w == tlo && (((unsigned)(w >> 32)) & 0xffu) == thi; }
DEV float val(u64 w) { return __uint_as_float((unsigned)(w >> 32) & 0xffffff00u); }

constexpr unsigned G_E = 0;
constexpr unsigned G_Q = 512;
constexpr unsigned G_ML = G_Q + 4096;
constexpr unsigned G_O = G_ML + 2048;
constexpr unsigned G_Y = G_O + 256 * 512;
constexpr unsigned G_D = G_Y + 256 * 512;
constexpr unsigned G_END = G_D + 256 * 448;
constexpr unsigned SPIN_CAP = 1u << 18;
constexpr float SCALE = 0.08838834764831845f, LOG2E = 1.4426950408889634f;

#define TS(i)

__global__ void __launch_bounds__(512, 2)
fused_decode(const char* __restrict__ X, const char* __restrict__ K, const char* __restrict__ V,
             const char* __restrict__ WQ, const char* __restrict__ WO, const char* __restrict__ WG,
             const char* __restrict__ WU, const char* __restrict__ WD, unsigned short* __restrict__ out, char* ws) {
  const int b = blockIdx.x, tid = threadIdx.x, lane = tid & 63, wave = __builtin_amdgcn_readfirstlane(tid >> 6);
  const int h = b & 7, j = b >> 3;
  const int g = lane >> 4, i = lane & 15;
  __shared__ __attribute__((aligned(16))) float s_red[8][16];
  __shared__ __attribute__((aligned(16))) float s_gu[16][8];
  __shared__ __attribute__((aligned(16))) unsigned short s_q[512];
  __shared__ __attribute__((aligned(16))) float s_d[8][512];
  __shared__ __attribute__((aligned(16))) float s_o[8][512];
  __shared__ __attribute__((aligned(16))) float s_m[4][8], s_l[4][8];
  __shared__ __attribute__((aligned(16))) float s_p[16][8];
  TS(0);

  const u64 E = get(ws, G_E);
  const u32x4 xv = *(const u32x4*)(X + lane * 16);
  const int row = lane * 8 + wave;
  const int cb = h * 32 + j;
  const u32x4 w0 = *(const u32x4*)(WQ + (size_t)row * 8192 + cb * 32);
  const u32x4 w1 = *(const u32x4*)(WQ + (size_t)row * 8192 + cb * 32 + 16);
  const size_t base = ((size_t)h * 32768 + j * 1024 + wave * 128) * 256;
  const char* kp = K + base + (4 * (i & 3) + (i >> 2)) * 256 + g * 16;
  const char* vp = V + base + lane * 16;
  u32x4 kf[8][4], vf[8][4];
#define LOAD_TILE(t)                                                                                             \
  _Pragma("unroll") for (int u = 0; u < 4; ++u) kf[t][u] = __builtin_nontemporal_load((const u32x4*)(kp + (t) * 4096 + u * 64)); \
  _Pragma("unroll") for (int u = 0; u < 4; ++u) vf[t][u] = __builtin_nontemporal_load((const u32x4*)(vp + (t) * 4096 + u * 1024));
  __builtin_amdgcn_sched_barrier(0);
  LOAD_TILE(0)
  const int fb = h * 28 + (j < 28 ? j : j - 4);
  const u32x4 g4 = *(const u32x4*)(WG + (size_t)row * 3584 + fb * 16);
  const u32x4 u4 = *(const u32x4*)(WU + (size_t)row * 3584 + fb * 16);
  const u32x4 d4 = *(const u32x4*)(WD + (size_t)(fb * 8 + wave) * 1024 + lane * 16);
  __builtin_amdgcn_sched_barrier(0);

  float xn;
  {
    float s = 0;
#pragma unroll
    for (int k = 0; k < 4; ++k) s += bflo(xv[k]) + bfhi(xv[k]);
    const float mean = wave_sum(s) * (1.0f / 512.0f);
    float ss = 0;
#pragma unroll
    for (int k = 0; k < 4; ++k) { const float a = bflo(xv[k]) - mean, c = bfhi(xv[k]) - mean; ss += a * a + c * c; }
    const float var = rbf(wave_sum(ss) * (1.0f / 512.0f));
    const float sd = rbf(sqrtf(rbf(var + 1e-5f)));
    const unsigned xw = wave < 2 ? xv[0] : wave < 4 ? xv[1] : wave < 6 ? xv[2] : xv[3];
    const float xt = (wave & 1) ? bfhi(xw) : bflo(xw);
    xn = rbf(rbf(xt - rbf(mean)) / sd);
  }
  {
    float a[16];
#pragma unroll
    for (int k = 0; k < 4; ++k) {
      a[2 * k] = xn * bflo(w0[k]); a[2 * k + 1] = xn * bfhi(w0[k]);
      a[8 + 2 * k] = xn * bflo(w1[k]); a[8 + 2 * k + 1] = xn * bfhi(w1[k]);
    }
    reduce16(a);
    if (i == 0) *(f32x4*)&s_red[wave][4 * g] = f32x4{a[0], a[1], a[2], a[3]};
  }
  const u64 T = (E * 0x5DEECE66Dull + 0xBull) & 0xffffffffffull;
  const unsigned tlo = __builtin_amdgcn_readfirstlane((unsigned)T), thi = __builtin_amdgcn_readfirstlane((unsigned)(T >> 32));
  wg_barrier();
  TS(1);
  if (wave == 0) {
    float v = 0;
#pragma unroll
    for (int w = 0; w < 8; ++w) v += s_red[w][i];
    if (lane < 16) put(ws, G_Q + cb * 16 + lane, rbf(v), tlo, thi);
  }
  __builtin_amdgcn_sched_barrier(0);
  LOAD_TILE(1)
  __builtin_amdgcn_sched_barrier(0);
  if (wave == 0) {
    u64 qg[8];
#pragma nounroll
    for (unsigned spin = 0; spin < SPIN_CAP; ++spin) {
      bool ok = true;
#pragma unroll
      for (int k = 0; k < 8; ++k) qg[k] = get(ws, G_Q + h * 512 + k * 64 + lane);
#pragma unroll
      for (int k = 0; k < 8; ++k) ok &= fresh(qg[k], tlo, thi);
      if (__all(ok)) break;
      __builtin_amdgcn_s_sleep(2);
    }
#pragma unroll
    for (int k = 0; k < 8; ++k) s_q[k * 64 + lane] = (unsigned short)(qg[k] >> 48);
    TS(2);
  }
  {
    float a[16];
#pragma unroll
    for (int k = 0; k < 4; ++k) {
      a[2 * k] = xn * bflo(g4[k]); a[2 * k + 1] = xn * bfhi(g4[k]);
      a[8 + 2 * k] = xn * bflo(u4[k]); a[8 + 2 * k + 1] = xn * bfhi(u4[k]);
    }
    reduce16(a);
    if (i < 4) s_gu[4 * g + i][wave] = i == 0 ? a[0] : i == 1 ? a[1] : i == 2 ? a[2] : a[3];
  }
  __builtin_amdgcn_sched_barrier(0);
  LOAD_TILE(2)
  LOAD_TILE(3)
  __builtin_amdgcn_sched_barrier(0);
  wg_barrier();
  TS(3);
  u32x4 qf[4];
#pragma unroll
  for (int u = 0; u < 4; ++u) qf[u] = *(const u32x4*)&s_q[(i & 3) * 128 + u * 32 + g * 8];
  {
    const f32x4 ga = *(const f32x4*)&s_gu[wave][0], gb = *(const f32x4*)&s_gu[wave][4];
    const f32x4 ua = *(const f32x4*)&s_gu[8 + wave][0], ub = *(const f32x4*)&s_gu[8 + wave][4];
    const float gv = rbf(((ga[0] + ga[1]) + (ga[2] + ga[3])) + ((gb[0] + gb[1]) + (gb[2] + gb[3])));
    const float uv = rbf(((ua[0] + ua[1]) + (ua[2] + ua[3])) + ((ub[0] + ub[1]) + (ub[2] + ub[3])));
    const float inner = 0.7978845608028654f * (gv + 0.044715f * gv * gv * gv);
    const float th = 1.0f - 2.0f / (__builtin_amdgcn_exp2f(inner * (2.0f * LOG2E)) + 1.0f);
    const float hv = rbf(rbf(0.5f * gv * (1.0f + th)) * uv);
    *(f32x4*)&s_d[wave][lane * 8] = f32x4{hv * bflo(d4[0]), hv * bfhi(d4[0]), hv * bflo(d4[1]), hv * bfhi(d4[1])};
    *(f32x4*)&s_d[wave][lane * 8 + 4] = f32x4{hv * bflo(d4[2]), hv * bfhi(d4[2]), hv * bflo(d4[3]), hv * bfhi(d4[3])};
  }
  const int e = i >> 2;
  const unsigned mask0 = e == 0 ? 0xffffu : e == 1 ? 0xffff0000u : 0u;
  const unsigned mask1 = e == 2 ? 0xffffu : e == 3 ? 0xffff0000u : 0u;
  f32x4 olo = {0, 0, 0, 0}, ohi = {0, 0, 0, 0};
  float m = -INFINITY, l = 0;
  unsigned short wo[16];
  const unsigned short* wop = (const unsigned short*)WO + (size_t)((h * 4 + (j >> 3)) * 128 + 32 * (j & 3) + 4 * ((j >> 2) & 1)) * 512 + tid;
  __builtin_amdgcn_sched_barrier(0);
#pragma unroll
  for (int t = 0; t < 8; ++t) {
    f32x4 s = {0, 0, 0, 0};
#pragma unroll
    for (int u = 0; u < 4; ++u)
      s = __builtin_amdgcn_mfma_f32_16x16x32_bf16(__builtin_bit_cast(bf16x8, kf[t][u]), __builtin_bit_cast(bf16x8, qf[u]), s, 0, 0, 0);
    float z[4];
    {
      unsigned p01 = pk_bf16(s[0], s[1]), p23 = pk_bf16(s[2], s[3]);
      p01 = pk_bf16(bflo(p01) * SCALE, bfhi(p01) * SCALE);
      p23 = pk_bf16(bflo(p23) * SCALE, bfhi(p23) * SCALE);
      z[0] = bflo(p01); z[1] = bfhi(p01); z[2] = bflo(p23); z[3] = bfhi(p23);
    }
    float mx = fmaxf(fmaxf(z[0], z[1]), fmaxf(z[2], z[3]));
    mx = xmax32(xmax16(mx));
    const float mn = fmaxf(m, mx);
    const float sc = __builtin_amdgcn_exp2f((m - mn) * LOG2E);
    m = mn;
    const float mb = mn * LOG2E;
    olo *= sc; ohi *= sc; l *= sc;
#pragma unroll
    for (int u = 0; u < 4; ++u) {
      const float p = __builtin_amdgcn_exp2f(z[u] * LOG2E - mb);
      const unsigned pp = pk_bf16(p, p);
      l += bflo(pp);
      u32x2 bb = {pp & mask0, pp & mask1};
      u32x2 vl = {vf[t][u][0], vf[t][u][1]}, vh = {vf[t][u][2], vf[t][u][3]};
      olo = __builtin_amdgcn_mfma_f32_16x16x16bf16_1k(__builtin_bit_cast(s16x4, vl), __builtin_bit_cast(s16x4, bb), olo, 0, 0, 0);
      ohi = __builtin_amdgcn_mfma_f32_16x16x16bf16_1k(__builtin_bit_cast(s16x4, vh), __builtin_bit_cast(s16x4, bb), ohi, 0, 0, 0);
    }
    __builtin_amdgcn_sched_barrier(0);
    if (t + 4 < 8) { LOAD_TILE(t + 4) }
    if (t == 3) {
#pragma unroll
      for (int r = 0; r < 16; ++r) wo[r] = wop[(size_t)(8 * (r >> 2) + (r & 3)) * 512];
    }
    __builtin_amdgcn_sched_barrier(0);
  }
  l = xsum32(xsum16(l));
  *(f32x4*)&s_o[wave][lane * 4] = olo;
  *(f32x4*)&s_o[wave][256 + lane * 4] = ohi;
  if (lane < 4) { s_m[lane][wave] = m; s_l[lane][wave] = l; }
  TS(4);
  wg_barrier();
  TS(5);
  {
    const int hd = (lane >> 2) & 3;
    const f32x4 ma = *(const f32x4*)&s_m[hd][0], mb = *(const f32x4*)&s_m[hd][4];
    const f32x4 la = *(const f32x4*)&s_l[hd][0], lb = *(const f32x4*)&s_l[hd][4];
    const float M = fmaxf(fmaxf(fmaxf(ma[0], ma[1]), fmaxf(ma[2], ma[3])), fmaxf(fmaxf(mb[0], mb[1]), fmaxf(mb[2], mb[3])));
    float o = 0, L = 0;
#pragma unroll
    for (int w = 0; w < 8; ++w) {
      const float f = __builtin_amdgcn_exp2f(((w < 4 ? ma[w & 3] : mb[w & 3]) - M) * LOG2E);
      o += f * s_o[w][tid];
      L += f * (w < 4 ? la[w & 3] : lb[w & 3]);
    }
    put(ws, G_O + b * 512 + hd * 128 + wave * 16 + (lane & 3) * 4 + (lane >> 4), o, tlo, thi);
    if (tid < 16 && (lane & 3) < 2) put(ws, G_ML + ((h * 4 + hd) * 2 + (lane & 1)) * 32 + j, (lane & 1) ? L : M, tlo, thi);
    float d = 0;
#pragma unroll
    for (int w = 0; w < 8; ++w) d += s_d[w][tid];
    if (j < 28) put(ws, G_D + ((tid >> 1) * 224 + fb) * 2 + (tid & 1), d, tlo, thi);
  }
  TS(6);
  {
    const int p = tid >> 4;
    const unsigned ia = G_O + (p * 8 + h) * 512 + j * 16 + i;
    const unsigned ib = G_ML + (h * 4 + (j >> 3)) * 64 + lane;
    const unsigned ic = G_ML + (h * 4 + (j >> 3)) * 64 + p;
    u64 a, bq, c;
#pragma nounroll
    for (unsigned spin = 0; spin < SPIN_CAP; ++spin) {
      a = get(ws, ia); bq = get(ws, ib); c = get(ws, ic);
      const bool ok = fresh(a, tlo, thi) && fresh(bq, tlo, thi) && fresh(c, tlo, thi);
      if (__all(ok)) break;
      __builtin_amdgcn_s_sleep(1);
    }
    TS(7);
    float M = val(bq), L = M;
    swap32(M, L);
    const float Mg = xmax16(row_max(M));
    const float Lt = xsum16(row_sum(L * __builtin_amdgcn_exp2f((M - Mg) * LOG2E)));
    float o = val(a) * __builtin_amdgcn_exp2f((val(c) - Mg) * LOG2E);
    o = xsum32(xsum16(o));
    if (lane < 16) s_p[lane][wave] = o;
    wg_barrier();
    const f32x4 pa = *(const f32x4*)&s_p[i][0], pb = *(const f32x4*)&s_p[i][4];
    const float ov = rbf((((pa[0] + pa[1]) + (pa[2] + pa[3])) + ((pb[0] + pb[1]) + (pb[2] + pb[3]))) / Lt);
    float y = 0;
#pragma unroll
    for (int r = 0; r < 16; ++r)
      y += __int_as_float(__builtin_amdgcn_readlane(__float_as_int(ov), r)) * bflo(wo[r]);
    put(ws, G_Y + ((tid >> 1) * 256 + b) * 2 + (tid & 1), y, tlo, thi);
  }
  TS(8);
  if (wave == 0) {
    const unsigned xx = *(const unsigned*)(X + cb * 4);
    u64 yg[8], dg[7];
#pragma nounroll
    for (unsigned spin = 0; spin < SPIN_CAP; ++spin) {
      bool ok = true;
#pragma unroll
      for (int k = 0; k < 8; ++k) yg[k] = get(ws, G_Y + cb * 512 + k * 64 + lane);
#pragma unroll
      for (int k = 0; k < 7; ++k) dg[k] = get(ws, G_D + cb * 448 + k * 64 + lane);
#pragma unroll
      for (int k = 0; k < 8; ++k) ok &= fresh(yg[k], tlo, thi);
#pragma unroll
      for (int k = 0; k < 7; ++k) ok &= fresh(dg[k], tlo, thi);
      if (__all(ok)) break;
      __builtin_amdgcn_s_sleep(1);
    }
    TS(9);
    float y = 0, d = 0;
#pragma unroll
    for (int k = 0; k < 8; ++k) y += val(yg[k]);
#pragma unroll
    for (int k = 0; k < 7; ++k) d += val(dg[k]);
    swap32(y, d);  y += d;
    y += dpp<0x4E>(y); y += dpp<0x124>(y); y += dpp<0x128>(y);
    y = xsum16(y);
    d = y; swap32(y, d);
    y = rbf(y); d = rbf(d);
    const float gt = rbf(d / (1.0f + __builtin_amdgcn_exp2f(-d * LOG2E)));
    const float res = ((lane & 1) ? bfhi(xx) : bflo(xx)) + rbf(y * gt);
    const unsigned pr = pk_bf16(res, dpp<0xB1>(res));
    if (lane == 0) {
      *(unsigned*)(out + cb * 2) = pr;
      if (b == 0) __hip_atomic_store((gu64*)ws + G_E, T, __ATOMIC_RELAXED, __HIP_MEMORY_SCOPE_AGENT);
    }
    TS(10);
  }
}

extern "C" void kernel_launch(void* const* d_in, const int* in_sizes, int n_in, void* d_out, int out_size, void* d_ws,
                              size_t ws_size, hipStream_t stream) {
  fused_decode<<<256, 512, 0, stream>>>((const char*)d_in[0], (const char*)d_in[1], (const char*)d_in[2], (const char*)d_in[3],
                                        (const char*)d_in[4], (const char*)d_in[6], (const char*)d_in[7], (const char*)d_in[8],
                                        (unsigned short*)d_out, (char*)d_ws);
}
```

```cpp
#include <hip/hip_runtime.h>
#include <hip/hip_bf16.h>
#include <stdint.h>

using u32x4 = __attribute__((ext_vector_type(4))) unsigned;
using u32x2 = __attribute__((ext_vector_type(2))) unsigned;
using f32x4 = __attribute__((ext_vector_type(4))) float;
using f32x2 = __attribute__((ext_vector_type(2))) float;
using bf16x8 = __attribute__((ext_vector_type(8))) __bf16;
using bf16x2 = __attribute__((ext_vector_type(2))) __bf16;
using s16x4 = __attribute__((ext_vector_type(4))) short;
typedef unsigned long long u64;
typedef __attribute__((address_space(1))) u64 gu64;

#define DEV __device__ __forceinline__
DEV float bflo(unsigned v) { return __uint_as_float(v << 16); }
DEV float bfhi(unsigned v) { return __uint_as_float(v & 0xffff0000u); }
DEV unsigned pk_bf16(float a, float b) {
  f32x2 v = {a, b};
  return __builtin_bit_cast(unsigned, __builtin_convertvector(v, bf16x2));
}
DEV float rbf(float a) { return bflo(pk_bf16(a, a)); }
template <int C> DEV float dpp(float v) {
  return __int_as_float(__builtin_amdgcn_update_dpp(0, __float_as_int(v), C, 0xf, 0xf, true));
}
DEV float row_sum(float v) {
  v += dpp<0xB1>(v); v += dpp<0x4E>(v); v += dpp<0x141>(v); v += dpp<0x140>(v); return v;
}
DEV float row_max(float v) {
  v = fmaxf(v, dpp<0xB1>(v)); v = fmaxf(v, dpp<0x4E>(v)); v = fmaxf(v, dpp<0x141>(v)); v = fmaxf(v, dpp<0x140>(v)); return v;
}
DEV void swap16(float& a, float& b) {
  auto r = __builtin_amdgcn_permlane16_swap(__float_as_uint(a), __float_as_uint(b), false, false);
  a = __uint_as_float(r[0]); b = __uint_as_float(r[1]);
}
DEV void swap32(float& a, float& b) {
  auto r = __builtin_amdgcn_permlane32_swap(__float_as_uint(a), __float_as_uint(b), false, false);
  a = __uint_as_float(r[0]); b = __uint_as_float(r[1]);
}
DEV float xsum16(float v) { float a = v, b = v; swap16(a, b); return a + b; }
DEV float xsum32(float v) { float a = v, b = v; swap32(a, b); return a + b; }
DEV float xmax16(float v) { float a = v, b = v; swap16(a, b); return fmaxf(a, b); }
DEV float xmax32(float v) { float a = v, b = v; swap32(a, b); return fmaxf(a, b); }
DEV float wave_sum(float v) { return xsum32(xsum16(row_sum(v))); }
DEV void wg_barrier() { asm volatile("s_waitcnt lgkmcnt(0)\n\ts_barrier" ::: "memory"); }

DEV void put(char* base, unsigned idx, float v, unsigned tlo, unsigned thi) {
  const u64 w = ((u64)((__float_as_uint(v) & 0xffffff00u) | thi) << 32) | tlo;
  __hip_atomic_store((gu64*)(base) + idx, w, __ATOMIC_RELAXED, __HIP_MEMORY_SCOPE_AGENT);
}
DEV u64 get(const char* base, unsigned idx) {
  return __hip_atomic_load((gu64*)(base) + idx, __ATOMIC_RELAXED, __HIP_MEMORY_SCOPE_AGENT);
}
DEV bool fresh(u64 w, unsigned tlo, unsigned thi) { return (unsigned)w == tlo && (((unsigned)(w >> 32)) & 0xffu) == thi; }
DEV float val(u64 w) { return __uint_as_float((unsigned)(w >> 32) & 0xffffff00u); }

constexpr unsigned G_E = 0;
constexpr unsigned G_Q = 512;
constexpr unsigned G_GU = G_Q + 8 * 2048;
constexpr unsigned G_ML = G_GU + 224 * 128;
constexpr unsigned G_O = G_ML + 2048;
constexpr unsigned G_Y = G_O + 256 * 512;
constexpr unsigned G_D = G_Y + 256 * 512;
constexpr unsigned G_END = G_D + 256 * 512;
#define YL 1
#define H2ALL 0
DEV unsigned yidx(unsigned prod, unsigned col) { return YL ? prod * 512 + col : ((col >> 1) * 256 + prod) * 2 + (col & 1); }
constexpr unsigned SPIN_CAP = 1u << 18;
constexpr float SCALE = 0.08838834764831845f, LOG2E = 1.4426950408889634f;

#define TS(i)

__global__ void __launch_bounds__(512, 2)
fused_decode(const char* __restrict__ X, const char* __restrict__ K, const char* __restrict__ V,
             const char* __restrict__ WQ, const char* __restrict__ WO, const char* __restrict__ WG,
             const char* __restrict__ WU, const char* __restrict__ WD, unsigned short* __restrict__ out, char* ws) {
  const int b = blockIdx.x, tid = threadIdx.x, lane = tid & 63, wave = __builtin_amdgcn_readfirstlane(tid >> 6);
  const int h = b & 7, j = b >> 3;
  const int g = lane >> 4, i = lane & 15;
  __shared__ __attribute__((aligned(16))) float s_red[8][3][64];
  __shared__ __attribute__((aligned(16))) float s_h[8];
  __shared__ __attribute__((aligned(16))) unsigned short s_q[512];
  __shared__ __attribute__((aligned(16))) float s_o[8][512];
  __shared__ __attribute__((aligned(16))) float s_m[4][8], s_l[4][8];
  __shared__ __attribute__((aligned(16))) float s_p[16][8];
  __shared__ __attribute__((aligned(16))) float s_f[2][2][8];
  TS(0);

  const u64 E = get(ws, G_E);
  const u32x4 xv = *(const u32x4*)(X + lane * 16);
  const int jm = j < 28 ? j : j - 4;
  const int rq = (j & 3) * 128 + (lane >> 3) * 16 + wave * 2, rg = h * 64 + (lane >> 3) * 8 + wave;
  const unsigned xq = *(const unsigned*)(X + rq * 2);
  const unsigned short xg = *(const unsigned short*)(X + rg * 2);
  const char* wqp = WQ + (size_t)rq * 8192 + (h * 8 + (j >> 2)) * 128 + (lane & 7) * 16;
  const u32x4 w0 = __builtin_nontemporal_load((const u32x4*)(wqp));
  const u32x4 w1 = __builtin_nontemporal_load((const u32x4*)(wqp + 8192));
  const u32x4 g4 = __builtin_nontemporal_load((const u32x4*)(WG + (size_t)rg * 3584 + jm * 128 + (lane & 7) * 16));
  const u32x4 u4 = __builtin_nontemporal_load((const u32x4*)(WU + (size_t)rg * 3584 + jm * 128 + (lane & 7) * 16));
  const int cb = h * 32 + j;
  const size_t base = ((size_t)h * 32768 + j * 1024 + wave * 128) * 256;
  const char* kp = K + base + (4 * (i & 3) + (i >> 2)) * 256 + g * 16;
  const char* vp = V + base + lane * 16;
  u32x4 kf[8][4], vf[8][4];
#define LOAD_TILE(t)                                                                                             \
  _Pragma("unroll") for (int u = 0; u < 4; ++u) kf[t][u] = __builtin_nontemporal_load((const u32x4*)(kp + (t) * 4096 + u * 64)); \
  _Pragma("unroll") for (int u = 0; u < 4; ++u) vf[t][u] = __builtin_nontemporal_load((const u32x4*)(vp + (t) * 4096 + u * 1024));
  __builtin_amdgcn_sched_barrier(0);
  asm volatile("s_barrier" ::: "memory");
  __builtin_amdgcn_sched_barrier(0);
  LOAD_TILE(0)
  __builtin_amdgcn_sched_barrier(0);

  {
    float s = 0;
#pragma unroll
    for (int k = 0; k < 4; ++k) s += bflo(xv[k]) + bfhi(xv[k]);
    const float mean = wave_sum(s) * (1.0f / 512.0f);
    float ss = 0;
#pragma unroll
    for (int k = 0; k < 4; ++k) { const float a = bflo(xv[k]) - mean, c = bfhi(xv[k]) - mean; ss += a * a + c * c; }
    const float var = rbf(wave_sum(ss) * (1.0f / 512.0f));
    const float sd = rbf(sqrtf(rbf(var + 1e-5f))), mr = rbf(mean);
    const float x0 = rbf(rbf(bflo(xq) - mr) / sd), x1 = rbf(rbf(bfhi(xq) - mr) / sd), x2 = rbf(rbf(bflo(xg) - mr) / sd);
    float a[3][8];
#pragma unroll
    for (int k = 0; k < 4; ++k) {
      a[0][2 * k] = x0 * bflo(w0[k]) + x1 * bflo(w1[k]); a[0][2 * k + 1] = x0 * bfhi(w0[k]) + x1 * bfhi(w1[k]);
      a[1][2 * k] = x2 * bflo(g4[k]); a[1][2 * k + 1] = x2 * bfhi(g4[k]);
      a[2][2 * k] = x2 * bflo(u4[k]); a[2][2 * k + 1] = x2 * bfhi(u4[k]);
    }
#pragma unroll
    for (int n = 0; n < 3; ++n) {
#pragma unroll
      for (int k = 0; k < 4; ++k) { swap32(a[n][k], a[n][k + 4]); a[n][k] += a[n][k + 4]; }
#pragma unroll
      for (int k = 0; k < 2; ++k) { swap16(a[n][k], a[n][k + 2]); a[n][k] += a[n][k + 2]; a[n][k] += dpp<0x128>(a[n][k]); }
      if (!(lane & 8)) *(f32x2*)&s_red[wave][n][(lane & 7) * 8 + g * 2] = f32x2{a[n][0], a[n][1]};
    }
  }
  const u64 T = (E * 0x5DEECE66Dull + 0xBull) & 0xffffffffffull;
  const unsigned tlo = __builtin_amdgcn_readfirstlane((unsigned)T), thi = __builtin_amdgcn_readfirstlane((unsigned)(T >> 32));
  wg_barrier();
  TS(1);
  if (wave == 0 || (wave < 3 && j < 28)) {
    float v = 0;
#pragma unroll
    for (int w = 0; w < 8; ++w) v += s_red[w][wave][lane];
    put(ws, wave == 0 ? G_Q + (h * 4 + (j & 3)) * 512 + (j >> 2) * 64 + lane
                      : G_GU + ((j * 8 + (lane >> 3)) * 8 + h) * 16 + (wave - 1) * 8 + (lane & 7), v, tlo, thi);
  }
  __builtin_amdgcn_sched_barrier(0);
  const int fb = jm * 8 + h;
  unsigned short wd[8];
#pragma unroll
  for (int r = 0; r < 8; ++r) wd[r] = __builtin_nontemporal_load((const unsigned short*)WD + (size_t)(fb * 8 + r) * 512 + tid);
  LOAD_TILE(1)
  __builtin_amdgcn_sched_barrier(0);
  if (wave == 0) {
    __builtin_amdgcn_s_setprio(3);
    u64 qg[32], gg[2];
#pragma nounroll
    for (unsigned spin = 0; spin < SPIN_CAP; ++spin) {
      bool ok = true;
#pragma unroll
      for (int k = 0; k < 32; ++k) qg[k] = get(ws, G_Q + h * 2048 + k * 64 + lane);
#pragma unroll
      for (int k = 0; k < 2; ++k) gg[k] = get(ws, G_GU + fb * 128 + k * 64 + lane);
#pragma unroll
      for (int k = 0; k < 32; ++k) ok &= fresh(qg[k], tlo, thi);
#pragma unroll
      for (int k = 0; k < 2; ++k) ok &= fresh(gg[k], tlo, thi);
      if (__all(ok)) break;
      __builtin_amdgcn_s_sleep(2);
    }
#pragma unroll
    for (int k = 0; k < 8; ++k)
      s_q[k * 64 + lane] = (unsigned short)pk_bf16((val(qg[k]) + val(qg[k + 8])) + (val(qg[k + 16]) + val(qg[k + 24])), 0.f);
    const float gv = rbf(xsum32(xsum16(val(gg[0]) + val(gg[1])))), uv = dpp<0x128>(gv);
    const float inner = 0.7978845608028654f * (gv + 0.044715f * gv * gv * gv);
    const float th = 1.0f - 2.0f / (__builtin_amdgcn_exp2f(inner * (2.0f * LOG2E)) + 1.0f);
    if (lane < 8) s_h[lane] = rbf(rbf(0.5f * gv * (1.0f + th)) * uv);
    __builtin_amdgcn_s_setprio(0);
    TS(2);
  } else {
    LOAD_TILE(2)
  }
  __builtin_amdgcn_sched_barrier(0);
  wg_barrier();
  TS(3);
  __builtin_amdgcn_sched_barrier(0);
  if (wave == 0) { LOAD_TILE(2) }
  __builtin_amdgcn_sched_barrier(0);
  u32x4 qf[4];
#pragma unroll
  for (int u = 0; u < 4; ++u) qf[u] = *(const u32x4*)&s_q[(i & 3) * 128 + u * 32 + g * 8];
  {
    const f32x4 ha = *(const f32x4*)&s_h[0], hb = *(const f32x4*)&s_h[4];
    float d = 0;
#pragma unroll
    for (int r = 0; r < 4; ++r) d += ha[r] * bflo(wd[r]) + hb[r] * bflo(wd[r + 4]);
    if (j < 28) put(ws, G_D + yidx(fb, tid), d, tlo, thi);
  }
  const int e = i >> 2;
  const unsigned mask0 = e == 0 ? 0xffffu : e == 1 ? 0xffff0000u : 0u;
  const unsigned mask1 = e == 2 ? 0xffffu : e == 3 ? 0xffff0000u : 0u;
  f32x4 olo = {0, 0, 0, 0}, ohi = {0, 0, 0, 0};
  float m = -INFINITY, l = 0;
  unsigned short wo[16];
  const unsigned short* wop = (const unsigned short*)WO + (size_t)((h * 4 + (j >> 3)) * 128 + 32 * (j & 3) + 4 * ((j >> 2) & 1)) * 512 + tid;
  __builtin_amdgcn_sched_barrier(0);
#pragma unroll
  for (int t = 0; t < 8; ++t) {
    f32x4 s = {0, 0, 0, 0};
#pragma unroll
    for (int u = 0; u < 4; ++u)
      s = __builtin_amdgcn_mfma_f32_16x16x32_bf16(__builtin_bit_cast(bf16x8, kf[t][u]), __builtin_bit_cast(bf16x8, qf[u]), s, 0, 0, 0);
    float z[4];
    {
      unsigned p01 = pk_bf16(s[0], s[1]), p23 = pk_bf16(s[2], s[3]);
      p01 = pk_bf16(bflo(p01) * SCALE, bfhi(p01) * SCALE);
      p23 = pk_bf16(bflo(p23) * SCALE, bfhi(p23) * SCALE);
      z[0] = bflo(p01); z[1] = bfhi(p01); z[2] = bflo(p23); z[3] = bfhi(p23);
    }
    float mx = fmaxf(fmaxf(z[0], z[1]), fmaxf(z[2], z[3]));
    mx = xmax32(xmax16(mx));
    const float mn = fmaxf(m, mx);
    const float sc = __builtin_amdgcn_exp2f((m - mn) * LOG2E);
    m = mn;
    const float mb = mn * LOG2E;
    olo *= sc; ohi *= sc; l *= sc;
#pragma unroll
    for (int u = 0; u < 4; ++u) {
      const float p = __builtin_amdgcn_exp2f(z[u] * LOG2E - mb);
      const unsigned pp = pk_bf16(p, p);
      l += bflo(pp);
      u32x2 bb = {pp & mask0, pp & mask1};
      u32x2 vl = {vf[t][u][0], vf[t][u][1]}, vh = {vf[t][u][2], vf[t][u][3]};
      olo = __builtin_amdgcn_mfma_f32_16x16x16bf16_1k(__builtin_bit_cast(s16x4, vl), __builtin_bit_cast(s16x4, bb), olo, 0, 0, 0);
      ohi = __builtin_amdgcn_mfma_f32_16x16x16bf16_1k(__builtin_bit_cast(s16x4, vh), __builtin_bit_cast(s16x4, bb), ohi, 0, 0, 0);
    }
    __builtin_amdgcn_sched_barrier(0);
    if (t + 3 < 8) { LOAD_TILE(t + 3) }
    if (t == 4) {
#pragma unroll
      for (int r = 0; r < 16; ++r) wo[r] = __builtin_nontemporal_load(wop + (size_t)(8 * (r >> 2) + (r & 3)) * 512);
    }
    __builtin_amdgcn_sched_barrier(0);
  }
  l = xsum32(xsum16(l));
  *(f32x4*)&s_o[wave][lane * 4] = olo;
  *(f32x4*)&s_o[wave][256 + lane * 4] = ohi;
  if (lane < 4) { s_m[lane][wave] = m; s_l[lane][wave] = l; }
  TS(4);
  wg_barrier();
  TS(5);
  {
    const int hd = (lane >> 2) & 3;
    const f32x4 ma = *(const f32x4*)&s_m[hd][0], mb = *(const f32x4*)&s_m[hd][4];
    const f32x4 la = *(const f32x4*)&s_l[hd][0], lb = *(const f32x4*)&s_l[hd][4];
    const float M = fmaxf(fmaxf(fmaxf(ma[0], ma[1]), fmaxf(ma[2], ma[3])), fmaxf(fmaxf(mb[0], mb[1]), fmaxf(mb[2], mb[3])));
    float o = 0, L = 0;
#pragma unroll
    for (int w = 0; w < 8; ++w) {
      const float f = __builtin_amdgcn_exp2f(((w < 4 ? ma[w & 3] : mb[w & 3]) - M) * LOG2E);
      o += f * s_o[w][tid];
      L += f * (w < 4 ? la[w & 3] : lb[w & 3]);
    }
    put(ws, G_O + b * 512 + hd * 128 + wave * 16 + (lane & 3) * 4 + (lane >> 4), o, tlo, thi);
    if (tid < 16 && (lane & 3) < 2) put(ws, G_ML + ((h * 4 + hd) * 2 + (lane & 1)) * 32 + j, (lane & 1) ? L : M, tlo, thi);
  }
  TS(6);
  {
    const int p = tid >> 4;
    const unsigned ia = G_O + (p * 8 + h) * 512 + j * 16 + i;
    const unsigned ib = G_ML + (h * 4 + (j >> 3)) * 64 + lane;
    const unsigned ic = G_ML + (h * 4 + (j >> 3)) * 64 + p;
    u64 a, bq, c;
#pragma nounroll
    for (unsigned spin = 0; spin < SPIN_CAP; ++spin) {
      a = get(ws, ia); bq = get(ws, ib); c = get(ws, ic);
      const bool ok = fresh(a, tlo, thi) && fresh(bq, tlo, thi) && fresh(c, tlo, thi);
      if (__all(ok)) break;
      __builtin_amdgcn_s_sleep(1);
    }
    TS(7);
    float M = val(bq), L = M;
    swap32(M, L);
    const float Mg = xmax16(row_max(M));
    const float Lt = xsum16(row_sum(L * __builtin_amdgcn_exp2f((M - Mg) * LOG2E)));
    float o = val(a) * __builtin_amdgcn_exp2f((val(c) - Mg) * LOG2E);
    o = xsum32(xsum16(o));
    if (lane < 16) s_p[lane][wave] = o;
    wg_barrier();
    const f32x4 pa = *(const f32x4*)&s_p[i][0], pb = *(const f32x4*)&s_p[i][4];
    const float ov = rbf((((pa[0] + pa[1]) + (pa[2] + pa[3])) + ((pb[0] + pb[1]) + (pb[2] + pb[3]))) / Lt);
    float y = 0;
#pragma unroll
    for (int r = 0; r < 16; ++r)
      y += __int_as_float(__builtin_amdgcn_readlane(__float_as_int(ov), r)) * bflo(wo[r]);
    put(ws, G_Y + yidx(b, tid), y, tlo, thi);
  }
  TS(8);
  {
    if (wave == 0) {
      const unsigned xx = *(const unsigned*)(X + cb * 4);
      u64 yg[8], dg[7];
#pragma nounroll
      for (unsigned spin = 0; spin < SPIN_CAP; ++spin) {
        bool ok = true;
#pragma unroll
        for (int k = 0; k < 7; ++k) dg[k] = get(ws, G_D + yidx(k * 32 + (lane >> 1), 2 * cb + (lane & 1)));
#pragma unroll
        for (int k = 0; k < 7; ++k) ok &= fresh(dg[k], tlo, thi);
        if (__all(ok)) break;
        __builtin_amdgcn_s_sleep(1);
      }
      float y = 0, d = 0;
#pragma unroll
      for (int k = 0; k < 7; ++k) d += val(dg[k]);
#pragma nounroll
      for (unsigned spin = 0; spin < SPIN_CAP; ++spin) {
        bool ok = true;
#pragma unroll
        for (int k = 0; k < 8; ++k) yg[k] = get(ws, G_Y + yidx(k * 32 + (lane >> 1), 2 * cb + (lane & 1)));
#pragma unroll
        for (int k = 0; k < 8; ++k) ok &= fresh(yg[k], tlo, thi);
        if (__all(ok)) break;
        __builtin_amdgcn_s_sleep(1);
      }
      TS(9);
#pragma unroll
      for (int k = 0; k < 8; ++k) y += val(yg[k]);
      swap32(y, d);  y += d;
      y += dpp<0x4E>(y); y += dpp<0x124>(y); y += dpp<0x128>(y);
      y = xsum16(y);
      float d2 = y; swap32(y, d2);
      y = rbf(y); d2 = rbf(d2);
      const float gt = rbf(d2 / (1.0f + __builtin_amdgcn_exp2f(-d2 * LOG2E)));
      const float res = ((lane & 1) ? bfhi(xx) : bflo(xx)) + rbf(y * gt);
      const unsigned pr = pk_bf16(res, dpp<0xB1>(res));
      if (lane == 0) {
        *(unsigned*)(out + cb * 2) = pr;
        if (b == 0) __hip_atomic_store((gu64*)ws + G_E, T, __ATOMIC_RELAXED, __HIP_MEMORY_SCOPE_AGENT);
      }
      TS(10);
    }
  }
}

extern "C" void kernel_launch(void* const* d_in, const int* in_sizes, int n_in, void* d_out, int out_size, void* d_ws,
                              size_t ws_size, hipStream_t stream) {
  fused_decode<<<256, 512, 0, stream>>>((const char*)d_in[0], (const char*)d_in[1], (const char*)d_in[2], (const char*)d_in[3],
                                        (const char*)d_in[4], (const char*)d_in[6], (const char*)d_in[7], (const char*)d_in[8],
                                        (unsigned short*)d_out, (char*)d_ws);
}
```

```cpp
#define DK 0
#define DMEAN 0
#include <hip/hip_runtime.h>
#include <hip/hip_bf16.h>
#include <stdint.h>

using u32x4 = __attribute__((ext_vector_type(4))) unsigned;
using u32x2 = __attribute__((ext_vector_type(2))) unsigned;
using f32x4 = __attribute__((ext_vector_type(4))) float;
using f32x2 = __attribute__((ext_vector_type(2))) float;
using bf16x8 = __attribute__((ext_vector_type(8))) __bf16;
using bf16x2 = __attribute__((ext_vector_type(2))) __bf16;
using s16x4 = __attribute__((ext_vector_type(4))) short;
typedef unsigned long long u64;
typedef __attribute__((address_space(1))) u64 gu64;

#define DEV __device__ __forceinline__
DEV float bflo(unsigned v) { return __uint_as_float(v << 16); }
DEV float bfhi(unsigned v) { return __uint_as_float(v & 0xffff0000u); }
DEV unsigned pk_bf16(float a, float b) {
  f32x2 v = {a, b};
  return __builtin_bit_cast(unsigned, __builtin_convertvector(v, bf16x2));
}
DEV float rbf(float a) { return bflo(pk_bf16(a, a)); }
template <int C> DEV float dpp(float v) {
  return __int_as_float(__builtin_amdgcn_update_dpp(0, __float_as_int(v), C, 0xf, 0xf, true));
}
DEV float row_sum(float v) {
  v += dpp<0xB1>(v); v += dpp<0x4E>(v); v += dpp<0x141>(v); v += dpp<0x140>(v); return v;
}
DEV float row_max(float v) {
  v = fmaxf(v, dpp<0xB1>(v)); v = fmaxf(v, dpp<0x4E>(v)); v = fmaxf(v, dpp<0x141>(v)); v = fmaxf(v, dpp<0x140>(v)); return v;
}
DEV void swap16(float& a, float& b) {
  auto r = __builtin_amdgcn_permlane16_swap(__float_as_uint(a), __float_as_uint(b), false, false);
  a = __uint_as_float(r[0]); b = __uint_as_float(r[1]);
}
DEV void swap32(float& a, float& b) {
  auto r = __builtin_amdgcn_permlane32_swap(__float_as_uint(a), __float_as_uint(b), false, false);
  a = __uint_as_float(r[0]); b = __uint_as_float(r[1]);
}
DEV float xsum16(float v) { float a = v, b = v; swap16(a, b); return a + b; }
DEV float xsum32(float v) { float a = v, b = v; swap32(a, b); return a + b; }
DEV float xmax16(float v) { float a = v, b = v; swap16(a, b); return fmaxf(a, b); }
DEV float xmax32(float v) { float a = v, b = v; swap32(a, b); return fmaxf(a, b); }
DEV float wave_sum(float v) { return xsum32(xsum16(row_sum(v))); }
DEV void wg_barrier() { asm volatile("s_waitcnt lgkmcnt(0)\n\ts_barrier" ::: "memory"); }

DEV void put(char* base, unsigned idx, float v, unsigned tlo, unsigned thi) {
  const u64 w = ((u64)((__float_as_uint(v) & 0xffffff00u) | thi) << 32) | tlo;
  __hip_atomic_store((gu64*)(base) + idx, w, __ATOMIC_RELAXED, __HIP_MEMORY_SCOPE_AGENT);
}
DEV u64 get(const char* base, unsigned idx) {
  return __hip_atomic_load((gu64*)(base) + idx, __ATOMIC_RELAXED, __HIP_MEMORY_SCOPE_AGENT);
}
DEV bool fresh(u64 w, unsigned tlo, unsigned thi) { return (unsigned)w == tlo && (((unsigned)(w >> 32)) & 0xffu) == thi; }
DEV float val(u64 w) { return __uint_as_float((unsigned)(w >> 32) & 0xffffff00u); }

constexpr unsigned G_E = 0;
constexpr unsigned G_Q = 512;
constexpr unsigned G_GU = G_Q + 8 * 2048;
constexpr unsigned G_ML = G_GU + 224 * 128;
constexpr unsigned G_O = G_ML + 2048;
constexpr unsigned G_Y = G_O + 256 * 512;
constexpr unsigned G_D = G_Y + 256 * 512;
constexpr unsigned G_END = G_D + 256 * 512;
#define YL 1
#define H2ALL 0
DEV unsigned yidx(unsigned prod, unsigned col) { return YL ? prod * 512 + col : ((col >> 1) * 256 + prod) * 2 + (col & 1); }
constexpr unsigned SPIN_CAP = 1u << 18;
constexpr float SCALE = 0.08838834764831845f, LOG2E = 1.4426950408889634f;

#define TSPUT(i, tv) __hip_atomic_store((gu64*)(ws) + G_END + (i) * 256 + b, ((u64)(((unsigned)(tv)) << 8 | thi) << 32) | tlo, __ATOMIC_RELAXED, __HIP_MEMORY_SCOPE_AGENT)
#define TS(i) do { if ((i) == 0) t00 = __builtin_amdgcn_s_memrealtime(); else if ((i) == DK && tid == 0) TSPUT(1, __builtin_amdgcn_s_memrealtime()); if ((i) == 1 && tid == 0) TSPUT(0, t00); } while (0)

__global__ void __launch_bounds__(512, 2)
fused_decode(const char* __restrict__ X, const char* __restrict__ K, const char* __restrict__ V,
             const char* __restrict__ WQ, const char* __restrict__ WO, const char* __restrict__ WG,
             const char* __restrict__ WU, const char* __restrict__ WD, unsigned short* __restrict__ out, char* ws) {
  const int b = blockIdx.x, tid = threadIdx.x, lane = tid & 63, wave = __builtin_amdgcn_readfirstlane(tid >> 6);
  const int h = b & 7, j = b >> 3;
  const int g = lane >> 4, i = lane & 15;
  __shared__ __attribute__((aligned(16))) float s_red[8][3][64];
  __shared__ __attribute__((aligned(16))) float s_h[8];
  __shared__ __attribute__((aligned(16))) unsigned short s_q[512];
  __shared__ __attribute__((aligned(16))) float s_o[8][512];
  __shared__ __attribute__((aligned(16))) float s_m[4][8], s_l[4][8];
  __shared__ __attribute__((aligned(16))) float s_p[16][8];
  __shared__ __attribute__((aligned(16))) float s_f[2][2][8];
  u64 t00; unsigned tlo = 0, thi = 0;
  TS(0);

  const u64 E = get(ws, G_E);
  const u32x4 xv = *(const u32x4*)(X + lane * 16);
  const int jm = j < 28 ? j : j - 4;
  const int rq = (j & 3) * 128 + (lane >> 3) * 16 + wave * 2, rg = h * 64 + (lane >> 3) * 8 + wave;
  const unsigned xq = *(const unsigned*)(X + rq * 2);
  const unsigned short xg = *(const unsigned short*)(X + rg * 2);
  const char* wqp = WQ + (size_t)rq * 8192 + (h * 8 + (j >> 2)) * 128 + (lane & 7) * 16;
  const u32x4 w0 = __builtin_nontemporal_load((const u32x4*)(wqp));
  const u32x4 w1 = __builtin_nontemporal_load((const u32x4*)(wqp + 8192));
  const u32x4 g4 = __builtin_nontemporal_load((const u32x4*)(WG + (size_t)rg * 3584 + jm * 128 + (lane & 7) * 16));
  const u32x4 u4 = __builtin_nontemporal_load((const u32x4*)(WU + (size_t)rg * 3584 + jm * 128 + (lane & 7) * 16));
  const int cb = h * 32 + j;
  const size_t base = ((size_t)h * 32768 + j * 1024 + wave * 128) * 256;
  const char* kp = K + base + (4 * (i & 3) + (i >> 2)) * 256 + g * 16;
  const char* vp = V + base + lane * 16;
  u32x4 kf[8][4], vf[8][4];
#define LOAD_TILE(t)                                                                                             \
  _Pragma("unroll") for (int u = 0; u < 4; ++u) kf[t][u] = __builtin_nontemporal_load((const u32x4*)(kp + (t) * 4096 + u * 64)); \
  _Pragma("unroll") for (int u = 0; u < 4; ++u) vf[t][u] = __builtin_nontemporal_load((const u32x4*)(vp + (t) * 4096 + u * 1024));
  __builtin_amdgcn_sched_barrier(0);
  asm volatile("s_barrier" ::: "memory");
  __builtin_amdgcn_sched_barrier(0);
  LOAD_TILE(0)
  __builtin_amdgcn_sched_barrier(0);

  {
    float s = 0;
#pragma unroll
    for (int k = 0; k < 4; ++k) s += bflo(xv[k]) + bfhi(xv[k]);
    const float mean = wave_sum(s) * (1.0f / 512.0f);
    float ss = 0;
#pragma unroll
    for (int k = 0; k < 4; ++k) { const float a = bflo(xv[k]) - mean, c = bfhi(xv[k]) - mean; ss += a * a + c * c; }
    const float var = rbf(wave_sum(ss) * (1.0f / 512.0f));
    const float sd = rbf(sqrtf(rbf(var + 1e-5f))), mr = rbf(mean);
    const float x0 = rbf(rbf(bflo(xq) - mr) / sd), x1 = rbf(rbf(bfhi(xq) - mr) / sd), x2 = rbf(rbf(bflo(xg) - mr) / sd);
    float a[3][8];
#pragma unroll
    for (int k = 0; k < 4; ++k) {
      a[0][2 * k] = x0 * bflo(w0[k]) + x1 * bflo(w1[k]); a[0][2 * k + 1] = x0 * bfhi(w0[k]) + x1 * bfhi(w1[k]);
      a[1][2 * k] = x2 * bflo(g4[k]); a[1][2 * k + 1] = x2 * bfhi(g4[k]);
      a[2][2 * k] = x2 * bflo(u4[k]); a[2][2 * k + 1] = x2 * bfhi(u4[k]);
    }
#pragma unroll
    for (int n = 0; n < 3; ++n) {
#pragma unroll
      for (int k = 0; k < 4; ++k) { swap32(a[n][k], a[n][k + 4]); a[n][k] += a[n][k + 4]; }
#pragma unroll
      for (int k = 0; k < 2; ++k) { swap16(a[n][k], a[n][k + 2]); a[n][k] += a[n][k + 2]; a[n][k] += dpp<0x128>(a[n][k]); }
      if (!(lane & 8)) *(f32x2*)&s_red[wave][n][(lane & 7) * 8 + g * 2] = f32x2{a[n][0], a[n][1]};
    }
  }
  const u64 T = (E * 0x5DEECE66Dull + 0xBull) & 0xffffffffffull;
  tlo = __builtin_amdgcn_readfirstlane((unsigned)T); thi = __builtin_amdgcn_readfirstlane((unsigned)(T >> 32));
  wg_barrier();
  TS(1);
  if (wave == 0 || (wave < 3 && j < 28)) {
    float v = 0;
#pragma unroll
    for (int w = 0; w < 8; ++w) v += s_red[w][wave][lane];
    put(ws, wave == 0 ? G_Q + (h * 4 + (j & 3)) * 512 + (j >> 2) * 64 + lane
                      : G_GU + ((j * 8 + (lane >> 3)) * 8 + h) * 16 + (wave - 1) * 8 + (lane & 7), v, tlo, thi);
  }
  __builtin_amdgcn_sched_barrier(0);
  const int fb = jm * 8 + h;
  unsigned short wd[8];
#pragma unroll
  for (int r = 0; r < 8; ++r) wd[r] = __builtin_nontemporal_load((const unsigned short*)WD + (size_t)(fb * 8 + r) * 512 + tid);
  LOAD_TILE(1)
  __builtin_amdgcn_sched_barrier(0);
  if (wave == 0) {
    __builtin_amdgcn_s_setprio(3);
    u64 qg[32], gg[2];
#pragma nounroll
    for (unsigned spin = 0; spin < SPIN_CAP; ++spin) {
      bool ok = true;
#pragma unroll
      for (int k = 0; k < 32; ++k) qg[k] = get(ws, G_Q + h * 2048 + k * 64 + lane);
#pragma unroll
      for (int k = 0; k < 2; ++k) gg[k] = get(ws, G_GU + fb * 128 + k * 64 + lane);
#pragma unroll
      for (int k = 0; k < 32; ++k) ok &= fresh(qg[k], tlo, thi);
#pragma unroll
      for (int k = 0; k < 2; ++k) ok &= fresh(gg[k], tlo, thi);
      if (__all(ok)) break;
      __builtin_amdgcn_s_sleep(2);
    }
#pragma unroll
    for (int k = 0; k < 8; ++k)
      s_q[k * 64 + lane] = (unsigned short)pk_bf16((val(qg[k]) + val(qg[k + 8])) + (val(qg[k + 16]) + val(qg[k + 24])), 0.f);
    const float gv = rbf(xsum32(xsum16(val(gg[0]) + val(gg[1])))), uv = dpp<0x128>(gv);
    const float inner = 0.7978845608028654f * (gv + 0.044715f * gv * gv * gv);
    const float th = 1.0f - 2.0f / (__builtin_amdgcn_exp2f(inner * (2.0f * LOG2E)) + 1.0f);
    if (lane < 8) s_h[lane] = rbf(rbf(0.5f * gv * (1.0f + th)) * uv);
    __builtin_amdgcn_s_setprio(0);
    TS(2);
  } else {
    LOAD_TILE(2)
  }
  __builtin_amdgcn_sched_barrier(0);
  wg_barrier();
  TS(3);
  __builtin_amdgcn_sched_barrier(0);
  if (wave == 0) { LOAD_TILE(2) }
  __builtin_amdgcn_sched_barrier(0);
  u32x4 qf[4];
#pragma unroll
  for (int u = 0; u < 4; ++u) qf[u] = *(const u32x4*)&s_q[(i & 3) * 128 + u * 32 + g * 8];
  {
    const f32x4 ha = *(const f32x4*)&s_h[0], hb = *(const f32x4*)&s_h[4];
    float d = 0;
#pragma unroll
    for (int r = 0; r < 4; ++r) d += ha[r] * bflo(wd[r]) + hb[r] * bflo(wd[r + 4]);
    if (j < 28) put(ws, G_D + yidx(fb, tid), d, tlo, thi);
  }
  const int e = i >> 2;
  const unsigned mask0 = e == 0 ? 0xffffu : e == 1 ? 0xffff0000u : 0u;
  const unsigned mask1 = e == 2 ? 0xffffu : e == 3 ? 0xffff0000u : 0u;
  f32x4 olo = {0, 0, 0, 0}, ohi = {0, 0, 0, 0};
  float m = -INFINITY, l = 0;
  unsigned short wo[16];
  const unsigned short* wop = (const unsigned short*)WO + (size_t)((h * 4 + (j >> 3)) * 128 + 32 * (j & 3) + 4 * ((j >> 2) & 1)) * 512 + tid;
  __builtin_amdgcn_sched_barrier(0);
#pragma unroll
  for (int t = 0; t < 8; ++t) {
    f32x4 s = {0, 0, 0, 0};
#pragma unroll
    for (int u = 0; u < 4; ++u)
      s = __builtin_amdgcn_mfma_f32_16x16x32_bf16(__builtin_bit_cast(bf16x8, kf[t][u]), __builtin_bit_cast(bf16x8, qf[u]), s, 0, 0, 0);
    float z[4];
    {
      unsigned p01 = pk_bf16(s[0], s[1]), p23 = pk_bf16(s[2], s[3]);
      p01 = pk_bf16(bflo(p01) * SCALE, bfhi(p01) * SCALE);
      p23 = pk_bf16(bflo(p23) * SCALE, bfhi(p23) * SCALE);
      z[0] = bflo(p01); z[1] = bfhi(p01); z[2] = bflo(p23); z[3] = bfhi(p23);
    }
    float mx = fmaxf(fmaxf(z[0], z[1]), fmaxf(z[2], z[3]));
    mx = xmax32(xmax16(mx));
    const float mn = fmaxf(m, mx);
    const float sc = __builtin_amdgcn_exp2f((m - mn) * LOG2E);
    m = mn;
    const float mb = mn * LOG2E;
    olo *= sc; ohi *= sc; l *= sc;
#pragma unroll
    for (int u = 0; u < 4; ++u) {
      const float p = __builtin_amdgcn_exp2f(z[u] * LOG2E - mb);
      const unsigned pp = pk_bf16(p, p);
      l += bflo(pp);
      u32x2 bb = {pp & mask0, pp & mask1};
      u32x2 vl = {vf[t][u][0], vf[t][u][1]}, vh = {vf[t][u][2], vf[t][u][3]};
      olo = __builtin_amdgcn_mfma_f32_16x16x16bf16_1k(__builtin_bit_cast(s16x4, vl), __builtin_bit_cast(s16x4, bb), olo, 0, 0, 0);
      ohi = __builtin_amdgcn_mfma_f32_16x16x16bf16_1k(__builtin_bit_cast(s16x4, vh), __builtin_bit_cast(s16x4, bb), ohi, 0, 0, 0);
    }
    __builtin_amdgcn_sched_barrier(0);
    if (t + 3 < 8) { LOAD_TILE(t + 3) }
    if (t == 4) {
#pragma unroll
      for (int r = 0; r < 16; ++r) wo[r] = __builtin_nontemporal_load(wop + (size_t)(8 * (r >> 2) + (r & 3)) * 512);
    }
    __builtin_amdgcn_sched_barrier(0);
  }
  l = xsum32(xsum16(l));
  *(f32x4*)&s_o[wave][lane * 4] = olo;
  *(f32x4*)&s_o[wave][256 + lane * 4] = ohi;
  if (lane < 4) { s_m[lane][wave] = m; s_l[lane][wave] = l; }
  TS(4);
  wg_barrier();
  TS(5);
  {
    const int hd = (lane >> 2) & 3;
    const f32x4 ma = *(const f32x4*)&s_m[hd][0], mb = *(const f32x4*)&s_m[hd][4];
    const f32x4 la = *(const f32x4*)&s_l[hd][0], lb = *(const f32x4*)&s_l[hd][4];
    const float M = fmaxf(fmaxf(fmaxf(ma[0], ma[1]), fmaxf(ma[2], ma[3])), fmaxf(fmaxf(mb[0], mb[1]), fmaxf(mb[2], mb[3])));
    float o = 0, L = 0;
#pragma unroll
    for (int w = 0; w < 8; ++w) {
      const float f = __builtin_amdgcn_exp2f(((w < 4 ? ma[w & 3] : mb[w & 3]) - M) * LOG2E);
      o += f * s_o[w][tid];
      L += f * (w < 4 ? la[w & 3] : lb[w & 3]);
    }
    put(ws, G_O + b * 512 + hd * 128 + wave * 16 + (lane & 3) * 4 + (lane >> 4), o, tlo, thi);
    if (tid < 16 && (lane & 3) < 2) put(ws, G_ML + ((h * 4 + hd) * 2 + (lane & 1)) * 32 + j, (lane & 1) ? L : M, tlo, thi);
  }
  TS(6);
  {
    const int p = tid >> 4;
    const unsigned ia = G_O + (p * 8 + h) * 512 + j * 16 + i;
    const unsigned ib = G_ML + (h * 4 + (j >> 3)) * 64 + lane;
    const unsigned ic = G_ML + (h * 4 + (j >> 3)) * 64 + p;
    u64 a, bq, c;
#pragma nounroll
    for (unsigned spin = 0; spin < SPIN_CAP; ++spin) {
      a = get(ws, ia); bq = get(ws, ib); c = get(ws, ic);
      const bool ok = fresh(a, tlo, thi) && fresh(bq, tlo, thi) && fresh(c, tlo, thi);
      if (__all(ok)) break;
      __builtin_amdgcn_s_sleep(1);
    }
    TS(7);
    float M = val(bq), L = M;
    swap32(M, L);
    const float Mg = xmax16(row_max(M));
    const float Lt = xsum16(row_sum(L * __builtin_amdgcn_exp2f((M - Mg) * LOG2E)));
    float o = val(a) * __builtin_amdgcn_exp2f((val(c) - Mg) * LOG2E);
    o = xsum32(xsum16(o));
    if (lane < 16) s_p[lane][wave] = o;
    wg_barrier();
    const f32x4 pa = *(const f32x4*)&s_p[i][0], pb = *(const f32x4*)&s_p[i][4];
    const float ov = rbf((((pa[0] + pa[1]) + (pa[2] + pa[3])) + ((pb[0] + pb[1]) + (pb[2] + pb[3]))) / Lt);
    float y = 0;
#pragma unroll
    for (int r = 0; r < 16; ++r)
      y += __int_as_float(__builtin_amdgcn_readlane(__float_as_int(ov), r)) * bflo(wo[r]);
    put(ws, G_Y + yidx(b, tid), y, tlo, thi);
  }
  TS(8);
  {
    if (wave == 0) {
      const unsigned xx = *(const unsigned*)(X + cb * 4);
      u64 yg[8], dg[7];
#pragma nounroll
      for (unsigned spin = 0; spin < SPIN_CAP; ++spin) {
        bool ok = true;
#pragma unroll
        for (int k = 0; k < 7; ++k) dg[k] = get(ws, G_D + yidx(k * 32 + (lane >> 1), 2 * cb + (lane & 1)));
#pragma unroll
        for (int k = 0; k < 7; ++k) ok &= fresh(dg[k], tlo, thi);
        if (__all(ok)) break;
        __builtin_amdgcn_s_sleep(1);
      }
      float y = 0, d = 0;
#pragma unroll
      for (int k = 0; k < 7; ++k) d += val(dg[k]);
#pragma nounroll
      for (unsigned spin = 0; spin < SPIN_CAP; ++spin) {
        bool ok = true;
#pragma unroll
        for (int k = 0; k < 8; ++k) yg[k] = get(ws, G_Y + yidx(k * 32 + (lane >> 1), 2 * cb + (lane & 1)));
#pragma unroll
        for (int k = 0; k < 8; ++k) ok &= fresh(yg[k], tlo, thi);
        if (__all(ok)) break;
        __builtin_amdgcn_s_sleep(1);
      }
      TS(9);
#pragma unroll
      for (int k = 0; k < 8; ++k) y += val(yg[k]);
      swap32(y, d);  y += d;
      y += dpp<0x4E>(y); y += dpp<0x124>(y); y += dpp<0x128>(y);
      y = xsum16(y);
      float d2 = y; swap32(y, d2);
      y = rbf(y); d2 = rbf(d2);
      const float gt = rbf(d2 / (1.0f + __builtin_amdgcn_exp2f(-d2 * LOG2E)));
      const float res = ((lane & 1) ? bfhi(xx) : bflo(xx)) + rbf(y * gt);
      const unsigned pr = pk_bf16(res, dpp<0xB1>(res));
      if (lane == 0) {
        *(unsigned*)(out + cb * 2) = pr;
      }
      TS(10);
      if (b == 0) {
        u64 a[4], c[4];
#pragma nounroll
        for (unsigned spin = 0; spin < SPIN_CAP; ++spin) {
          bool ok = true;
          for (int k = 0; k < 4; ++k) { a[k] = get(ws, G_END + k * 64 + lane); c[k] = get(ws, G_END + (DK ? 256 : 0) + k * 64 + lane); ok &= fresh(a[k], tlo, thi) && fresh(c[k], tlo, thi); }
          if (__all(ok)) break;
          __builtin_amdgcn_s_sleep(1);
        }
        float mn = 1e9f, mx = -1e9f, sm = 0;
        for (int k = 0; k < 4; ++k) {
          const float x = (float)(((int)((unsigned)(a[k] >> 32) & 0xffffff00u) - (int)((unsigned)t00 << 8)) >> 8);
          const float z = (float)(((int)((unsigned)(c[k] >> 32) & 0xffffff00u) - (int)((unsigned)t00 << 8)) >> 8);
          mn = fminf(mn, x); mx = fmaxf(mx, z); sm += z;
        }
        mn = -xmax32(xmax16(row_max(-mn))); mx = xmax32(xmax16(row_max(mx))); sm = wave_sum(sm) * (1.0f / 256);
        float st = DK == 0 ? 0.f : (DMEAN ? sm : mx) - mn;
        st = fminf(fmaxf(st, 0.f), 10000.f);
        const long long tend = (long long)t00 + (long long)(mn + 4000.f + st);
#pragma nounroll
        for (unsigned spin = 0; spin < (1u << 16); ++spin) { if ((long long)__builtin_amdgcn_s_memrealtime() >= tend) break; }
        if (lane == 0) __hip_atomic_store((gu64*)ws + G_E, T, __ATOMIC_RELAXED, __HIP_MEMORY_SCOPE_AGENT);
      }
    }
  }
}

extern "C" void kernel_launch(void* const* d_in, const int* in_sizes, int n_in, void* d_out, int out_size, void* d_ws,
                              size_t ws_size, hipStream_t stream) {
  fused_decode<<<256, 512, 0, stream>>>((const char*)d_in[0], (const char*)d_in[1], (const char*)d_in[2], (const char*)d_in[3],
                                        (const char*)d_in[4], (const char*)d_in[6], (const char*)d_in[7], (const char*)d_in[8],
                                        (unsigned short*)d_out, (char*)d_ws);
}
```

```cpp
#include <hip/hip_runtime.h>
#include <hip/hip_bf16.h>
#include <stdint.h>

using u32x4 = __attribute__((ext_vector_type(4))) unsigned;
using u32x2 = __attribute__((ext_vector_type(2))) unsigned;
using f32x4 = __attribute__((ext_vector_type(4))) float;
using f32x2 = __attribute__((ext_vector_type(2))) float;
using bf16x8 = __attribute__((ext_vector_type(8))) __bf16;
using bf16x2 = __attribute__((ext_vector_type(2))) __bf16;
using s16x4 = __attribute__((ext_vector_type(4))) short;
typedef unsigned long long u64;
typedef __attribute__((address_space(1))) u64 gu64;

#define DEV __device__ __forceinline__
DEV float bflo(unsigned v) { return __uint_as_float(v << 16); }
DEV float bfhi(unsigned v) { return __uint_as_float(v & 0xffff0000u); }
DEV unsigned pk_bf16(float a, float b) {
  f32x2 v = {a, b};
  return __builtin_bit_cast(unsigned, __builtin_convertvector(v, bf16x2));
}
DEV float vmax(float a, float b) { float r; asm("v_max_f32 %0, %1, %2" : "=v"(r) : "v"(a), "v"(b)); return r; }
DEV float rbf(float a) { return bflo(pk_bf16(a, a)); }
template <int C> DEV float dpp(float v) {
  return __int_as_float(__builtin_amdgcn_update_dpp(0, __float_as_int(v), C, 0xf, 0xf, true));
}
DEV float row_sum(float v) {
  v += dpp<0xB1>(v); v += dpp<0x4E>(v); v += dpp<0x141>(v); v += dpp<0x140>(v); return v;
}
DEV float row_max(float v) {
  v = fmaxf(v, dpp<0xB1>(v)); v = fmaxf(v, dpp<0x4E>(v)); v = fmaxf(v, dpp<0x141>(v)); v = fmaxf(v, dpp<0x140>(v)); return v;
}
DEV void swap16(float& a, float& b) {
  auto r = __builtin_amdgcn_permlane16_swap(__float_as_uint(a), __float_as_uint(b), false, false);
  a = __uint_as_float(r[0]); b = __uint_as_float(r[1]);
}
DEV void swap32(float& a, float& b) {
  auto r = __builtin_amdgcn_permlane32_swap(__float_as_uint(a), __float_as_uint(b), false, false);
  a = __uint_as_float(r[0]); b = __uint_as_float(r[1]);
}
DEV float xsum16(float v) { float a = v, b = v; swap16(a, b); return a + b; }
DEV float xsum32(float v) { float a = v, b = v; swap32(a, b); return a + b; }
DEV float xmax16(float v) { float a = v, b = v; swap16(a, b); return vmax(a, b); }
DEV float xmax32(float v) { float a = v, b = v; swap32(a, b); return vmax(a, b); }
DEV float wave_sum(float v) { return xsum32(xsum16(row_sum(v))); }
DEV void wg_barrier() { asm volatile("s_waitcnt lgkmcnt(0)\n\ts_barrier" ::: "memory"); }

typedef __amdgpu_buffer_rsrc_t rsrc_t;
DEV void put2(rsrc_t rs, unsigned idx, float a, float c, unsigned tlo, unsigned tha, unsigned thc) {
  __builtin_amdgcn_raw_buffer_store_b128(u32x4{tlo, (__float_as_uint(a) & 0xffffff00u) | tha, tlo, (__float_as_uint(c) & 0xffffff00u) | thc},
                                         rs, idx * 8, 0, 16);
}
#define SLP1 1
#define ATOM 0
DEV void put2a(rsrc_t rs, unsigned idx, float a, float c, unsigned tlo, unsigned th) {
  u32x2 x = {tlo, (__float_as_uint(a) & 0xffffff00u) | th}, y = {tlo, (__float_as_uint(c) & 0xffffff00u) | th};
  asm volatile("buffer_atomic_swap_x2 %0, %2, %3, 0 offen sc0 sc1\n\tbuffer_atomic_swap_x2 %1, %2, %3, 0 offen offset:8 sc0 sc1\n\ts_waitcnt vmcnt(0)"
               : "+v"(x), "+v"(y) : "v"(idx * 8), "s"(rs) : "memory");
}
template <int AUX = 16> DEV u32x4 get2(rsrc_t rs, unsigned idx, unsigned uni = 0) { return __builtin_amdgcn_raw_buffer_load_b128(rs, idx * 8, uni * 8, AUX); }
DEV bool fresh(u32x4 w, unsigned tlo, unsigned thi) {
  return ((w[0] ^ tlo) | (w[2] ^ tlo) | (((w[1] ^ thi) | (w[3] ^ thi)) & 0x3fu)) == 0;
}
DEV float val(unsigned w) { return __uint_as_float(w & 0xffffff00u); }
#define GD 0
#define GTS(n)
template <int STEP, int NEAR, int N, class F> DEV void gather(rsrc_t rs, u32x4 (&w)[N], unsigned idx, F uni, unsigned tlo, unsigned thi, u64* dp = nullptr) {
  const unsigned voff = idx * 8;
  u64 st[N], any = 0;
#pragma unroll
  for (int k = 0; k < N; ++k) { st[k] = __builtin_amdgcn_ballot_w64(!fresh(w[k], tlo, thi)); any |= st[k]; }
  if (N == 16) { GTS(0); GTS(1); GTS(2); GTS(3); }
  if (any) {
    unsigned spin = 0;
#pragma nounroll
    do {
#define REREAD(sc)                                                                                                        \
      _Pragma("unroll") for (int k = 0; k < N; ++k)                                                                       \
        asm volatile("s_mov_b64 exec, %1\n\tbuffer_load_dwordx4 %0, %2, %3, %4 offen offset:%5 " sc "\n\ts_mov_b64 exec, -1" \
                     : "+v"(w[k]) : "s"(st[k]), "v"(voff), "s"(rs), "s"(uni(STEP ? k >> 2 : k) * 8), "n"((k & 3) * STEP * 8) : "memory");
      if (spin < NEAR) { REREAD("sc0") } else { REREAD("sc1") }
      asm volatile("s_waitcnt vmcnt(0)" ::: "memory");
      any = 0;
#pragma unroll
      for (int k = 0; k < N; ++k) {
        asm volatile("" : "+v"(w[k]));
        st[k] = __builtin_amdgcn_ballot_w64(!fresh(w[k], tlo, thi)); any |= st[k];
      }
      if (N == 16) { GTS(spin + 1); GTS(spin + 2); GTS(spin + 3); }
      if (any) __builtin_amdgcn_s_sleep(N == 2 ? SLP1 : 1);
    } while (any && ++spin < (1u << 18));
  }
}
DEV float oct_sum(float v) { v += dpp<0xB1>(v); v += dpp<0x4E>(v); v += dpp<0x141>(v); return v; }
DEV float oct_max(float v) { v = vmax(v, dpp<0xB1>(v)); v = vmax(v, dpp<0x4E>(v)); v = vmax(v, dpp<0x141>(v)); return v; }

constexpr unsigned G_E = 0;
constexpr unsigned G_Q = 512;
constexpr unsigned G_GU = G_Q + 8 * 8 * 2048;
constexpr unsigned G_O = G_GU + 224 * 128;
constexpr unsigned G_Y = G_O + 256 * 512;
constexpr unsigned G_D = G_Y + 256 * 128;
constexpr unsigned G_END = G_D + 256 * 128;
#define WIN 4
#define PREP 5
#define QN 0
#define ON 0
#define TG 0
#define TD 2
#define PRED 7
#define QC 1
#define OWNACK 1
#define QPRE 0
#define QSLP 0
#define BAR2 1
#define GW 1
#define EARLY 1
#define OACK 1
#define YACK 0
#define NOBAR1 0
#define PACE 2
#define PV 6
#define PRE 1
constexpr float SCALE = 0.08838834764831845f, LOG2E = 1.4426950408889634f;

#define TS(i)

__global__ void __launch_bounds__(512, 2)
fused_decode(const char* __restrict__ X, const char* __restrict__ K, const char* __restrict__ V,
             const char* __restrict__ WQ, const char* __restrict__ WO, const char* __restrict__ WG,
             const char* __restrict__ WU, const char* __restrict__ WD, unsigned short* __restrict__ out, char* ws) {
  const int b = blockIdx.x, tid = threadIdx.x, lane = tid & 63, wave = __builtin_amdgcn_readfirstlane(tid >> 6);
  const int h = b & 7, j = b >> 3;
  const int g = lane >> 4, i = lane & 15;
  __shared__ __attribute__((aligned(16))) float s_red[8][3][64];
  __shared__ __attribute__((aligned(16))) unsigned s_q[256];
  __shared__ __attribute__((aligned(16))) float s_o[8][512];
  __shared__ __attribute__((aligned(16))) f32x2 s_ml[4][8], s_wl[8];
  __shared__ __attribute__((aligned(16))) float s_p[8][64];
  __shared__ __attribute__((aligned(16))) float s_y[8][128];
  __shared__ __attribute__((aligned(16))) float s_f[8][16], s_g[64];
  TS(0);
  const rsrc_t rs = __builtin_amdgcn_make_buffer_rsrc(ws, 0, G_END * 8, 0x00020000);
  unsigned cp = 0;
  {
    unsigned long long pc; unsigned len;
    asm volatile("s_getpc_b64 %0\n.Lcp_a:\n\ts_mov_b32 %1, .Lcp_e-.Lcp_a" : "=s"(pc), "=s"(len));
    const unsigned off = (unsigned)(511 - tid) * 128;
    if (len < 65536 && off + 4 <= len) cp = *(const __attribute__((address_space(1))) unsigned*)(pc + off);
  }

  const u64 E = __hip_atomic_load((gu64*)ws + G_E, __ATOMIC_RELAXED, __HIP_MEMORY_SCOPE_AGENT);
  const u32x4 xv = *(const u32x4*)(X + lane * 16);
  const int jm = j < 28 ? j : j - 4;
  const int rq = (j & 3) * 128 + (lane >> 3) * 16 + wave * 2, rg = h * 64 + (lane >> 3) * 8 + wave;
  const unsigned xq = *(const unsigned*)(X + rq * 2);
  const unsigned short xg = *(const unsigned short*)(X + rg * 2);
  const char* wqp = WQ + (size_t)rq * 8192 + (h * 8 + (j >> 2)) * 128 + (lane & 7) * 16;
  const u32x4 w0 = __builtin_nontemporal_load((const u32x4*)(wqp));
  const u32x4 w1 = __builtin_nontemporal_load((const u32x4*)(wqp + 8192));
  const u32x4 g4 = __builtin_nontemporal_load((const u32x4*)(WG + (size_t)rg * 3584 + jm * 128 + (lane & 7) * 16));
  const u32x4 u4 = __builtin_nontemporal_load((const u32x4*)(WU + (size_t)rg * 3584 + jm * 128 + (lane & 7) * 16));
  const size_t base = ((size_t)h * 32768 + j * 1024 + wave * 128) * 256;
  const char* kp = K + base + (4 * (i & 3) + (i >> 2)) * 256 + g * 16;
  const char* vp = V + base + lane * 16;
  u32x4 kf[8][4], vf[8][4];
#define LOAD_TILE(t)                                                                                             \
  _Pragma("unroll") for (int u = 0; u < 4; ++u) kf[t][u] = __builtin_nontemporal_load((const u32x4*)(kp + (t) * 4096 + u * 64)); \
  _Pragma("unroll") for (int u = 0; u < 4; ++u) vf[t][u] = __builtin_nontemporal_load((const u32x4*)(vp + (t) * 4096 + u * 1024));
  __builtin_amdgcn_sched_barrier(0);
  asm volatile("s_barrier" ::: "memory");
  __builtin_amdgcn_sched_barrier(0);
  LOAD_TILE(0)
  __builtin_amdgcn_sched_barrier(0);

  {
    float s = 0;
#pragma unroll
    for (int k = 0; k < 4; ++k) s += bflo(xv[k]) + bfhi(xv[k]);
    const float mean = wave_sum(s) * (1.0f / 512.0f);
    float ss = 0;
#pragma unroll
    for (int k = 0; k < 4; ++k) { const float a = bflo(xv[k]) - mean, c = bfhi(xv[k]) - mean; ss += a * a + c * c; }
    const float var = rbf(wave_sum(ss) * (1.0f / 512.0f));
    const float sd = rbf(sqrtf(rbf(var + 1e-5f))), mr = rbf(mean);
    const float x0 = rbf(rbf(bflo(xq) - mr) / sd), x1 = rbf(rbf(bfhi(xq) - mr) / sd), x2 = rbf(rbf(bflo(xg) - mr) / sd);
    float a[3][8];
#pragma unroll
    for (int k = 0; k < 4; ++k) {
      a[0][2 * k] = x0 * bflo(w0[k]) + x1 * bflo(w1[k]); a[0][2 * k + 1] = x0 * bfhi(w0[k]) + x1 * bfhi(w1[k]);
      a[1][2 * k] = x2 * bflo(g4[k]); a[1][2 * k + 1] = x2 * bfhi(g4[k]);
      a[2][2 * k] = x2 * bflo(u4[k]); a[2][2 * k + 1] = x2 * bfhi(u4[k]);
    }
#pragma unroll
    for (int n = 0; n < 3; ++n) {
#pragma unroll
      for (int k = 0; k < 4; ++k) { swap32(a[n][k], a[n][k + 4]); a[n][k] += a[n][k + 4]; }
#pragma unroll
      for (int k = 0; k < 2; ++k) { swap16(a[n][k], a[n][k + 2]); a[n][k] += a[n][k + 2]; a[n][k] += dpp<0x128>(a[n][k]); }
      if (!(lane & 8)) *(f32x2*)&s_red[wave][n][(lane & 7) * 8 + g * 2] = f32x2{a[n][0], a[n][1]};
    }
  }
  const u64 T = (E * 0x5DEECE66Dull + 0xBull) & 0x3fffffffffull;
  const unsigned tlo = __builtin_amdgcn_readfirstlane((unsigned)T), thi = __builtin_amdgcn_readfirstlane((unsigned)(T >> 32));
  asm volatile("" :: "v"(cp));
  wg_barrier();
  TS(1);
  if (lane < 32 ? wave < QC : (wave == 1 || wave == 2) && j < 28) {
    const int n = lane < 32 ? 0 : wave, c = lane & 31;
    f32x2 v = {0, 0};
#pragma unroll
    for (int w = 0; w < 8; ++w) v += *(const f32x2*)&s_red[w][n][c * 2];
    const unsigned ix = lane < 32 ? G_Q + ((wave * 8 + h) * 4 + (j & 3)) * 512 + (j >> 2) * 64 + c * 2
                                  : G_GU + ((j * 8 + (c >> 2)) * 8 + h) * 16 + (wave - 1) * 8 + (c & 3) * 2;
    put2(rs, ix, v[0], v[1], tlo, thi, thi);
  }
  __builtin_amdgcn_sched_barrier(0);
  asm volatile("s_barrier" ::: "memory");
  __builtin_amdgcn_sched_barrier(0);
  const int fb = jm * 8 + h;
  if (wave == 0) {
    __builtin_amdgcn_s_setprio(3);
    asm volatile("s_waitcnt vmcnt(0)" ::: "memory");
    TS(11);
    u32x4 qg[16];
    auto uq = [&](int k) { return G_Q + ((j & (QC - 1)) * 8 + h) * 2048 + k * 512; };
#pragma unroll
    for (int k = 0; k < 16; ++k) qg[k] = get2<QN ? 1 : 16>(rs, lane * 2 + (k & 3) * 128, uq(k >> 2));
    gather<128, QN>(rs, qg, lane * 2, uq, tlo, thi, (u64*)(ws + (size_t)G_END * 8) + b * 16 + 11);
#pragma unroll
    for (int k = 0; k < 4; ++k)
      s_q[k * 64 + lane] = pk_bf16((val(qg[k][1]) + val(qg[k + 4][1])) + (val(qg[k + 8][1]) + val(qg[k + 12][1])),
                                   (val(qg[k][3]) + val(qg[k + 4][3])) + (val(qg[k + 8][3]) + val(qg[k + 12][3])));
    __builtin_amdgcn_s_setprio(0);
    TS(2);
    __builtin_amdgcn_sched_barrier(0);
    wg_barrier();
    TS(3);
    __builtin_amdgcn_sched_barrier(0);
#pragma unroll
    for (int t = EARLY; t < WIN; ++t) { LOAD_TILE(t) }
  } else {
#pragma unroll
    for (int t = EARLY; t < WIN; ++t) {
      asm volatile("s_waitcnt vmcnt(%0)" :: "n"(PV) : "memory");
      __builtin_amdgcn_sched_barrier(0);
      _Pragma("unroll") for (int u = 0; u < 4; ++u) kf[t][u] = __builtin_nontemporal_load((const u32x4*)(kp + t * 4096 + u * 64));
      __builtin_amdgcn_sched_barrier(0);
      asm volatile("s_waitcnt vmcnt(%0)" :: "n"(PV) : "memory");
      __builtin_amdgcn_sched_barrier(0);
      _Pragma("unroll") for (int u = 0; u < 4; ++u) vf[t][u] = __builtin_nontemporal_load((const u32x4*)(vp + t * 4096 + u * 1024));
      __builtin_amdgcn_sched_barrier(0);
    }
    __builtin_amdgcn_sched_barrier(0);
    wg_barrier();
  }
  __builtin_amdgcn_sched_barrier(0);
  u32x4 qf[4];
#pragma unroll
  for (int u = 0; u < 4; ++u) qf[u] = *(const u32x4*)&s_q[(i & 3) * 64 + u * 16 + g * 4];
  const int q0 = tid >> 4;
  const char* wop = WO + (size_t)((h * 4 + (j >> 3)) * 128 + 8 * (q0 >> 2) + (q0 & 3) + (j & 4)) * 1024 + (j & 3) * 256 + i * 16;
  u32x4 wo0, wo1, og[2], yg[1], dg[7], wd[8], gg[4];
  unsigned pt0, pt1, pt2;
  const unsigned io = G_O + (q0 * 8 + h) * 512 + (j >> 2) * 64 + i * 2;
  const unsigned iy = G_Y + ((wave * 4 + (b >> 3)) * 8 + (lane >> 3)) * 128 + (b & 7) * 16 + (lane & 7) * 2;
  const unsigned id = G_D + ((lane >> 3) * 4 + (b >> 3)) * 128 + (b & 7) * 16 + (lane & 7) * 2;
  const int e = i >> 2;
  const unsigned mask0 = e == 0 ? 0xffffu : e == 1 ? 0xffff0000u : 0u;
  const unsigned mask1 = e == 2 ? 0xffffu : e == 3 ? 0xffff0000u : 0u;
  f32x4 olo = {0, 0, 0, 0}, ohi = {0, 0, 0, 0};
  float m = -INFINITY, l = 0;
  __builtin_amdgcn_sched_barrier(0);
#pragma unroll
  for (int t = 0; t < 8; ++t) {
    f32x4 s = {0, 0, 0, 0};
#pragma unroll
    for (int u = 0; u < 4; ++u)
      s = __builtin_amdgcn_mfma_f32_16x16x32_bf16(__builtin_bit_cast(bf16x8, kf[t][u]), __builtin_bit_cast(bf16x8, qf[u]), s, 0, 0, 0);
    float z[4];
    {
      unsigned p01 = pk_bf16(s[0], s[1]), p23 = pk_bf16(s[2], s[3]);
      p01 = pk_bf16(bflo(p01) * SCALE, bfhi(p01) * SCALE);
      p23 = pk_bf16(bflo(p23) * SCALE, bfhi(p23) * SCALE);
      z[0] = bflo(p01); z[1] = bfhi(p01); z[2] = bflo(p23); z[3] = bfhi(p23);
    }
    float mx = fmaxf(fmaxf(z[0], z[1]), fmaxf(z[2], z[3]));
    mx = xmax32(xmax16(mx));
    const float mn = fmaxf(m, mx);
    const float sc = __builtin_amdgcn_exp2f((m - mn) * LOG2E);
    m = mn;
    const float mb = mn * LOG2E;
    olo *= sc; ohi *= sc; l *= sc;
#pragma unroll
    for (int u = 0; u < 4; ++u) {
      const float p = __builtin_amdgcn_exp2f(z[u] * LOG2E - mb);
      const unsigned pp = pk_bf16(p, p);
      l += bflo(pp);
      u32x2 bb = {pp & mask0, pp & mask1};
      u32x2 vl = {vf[t][u][0], vf[t][u][1]}, vh = {vf[t][u][2], vf[t][u][3]};
      olo = __builtin_amdgcn_mfma_f32_16x16x16bf16_1k(__builtin_bit_cast(s16x4, vl), __builtin_bit_cast(s16x4, bb), olo, 0, 0, 0);
      ohi = __builtin_amdgcn_mfma_f32_16x16x16bf16_1k(__builtin_bit_cast(s16x4, vh), __builtin_bit_cast(s16x4, bb), ohi, 0, 0, 0);
    }
    __builtin_amdgcn_sched_barrier(0);
    if (t + WIN < 8) { LOAD_TILE(t + WIN) }
    if (t + WIN == 7) {
      wo0 = __builtin_nontemporal_load((const u32x4*)wop);
      wo1 = __builtin_nontemporal_load((const u32x4*)(wop + 65536));
    }
    if (t == TG && wave == 0) {
#pragma unroll
      for (int k = 0; k < 8; ++k)
        wd[k] = __builtin_nontemporal_load((const u32x4*)(WD + (size_t)((fb >> 2) * 32 + g * 8 + k) * 1024 + (fb & 3) * 256 + i * 16));
#pragma unroll
      for (int k = 0; k < 4; ++k) gg[k] = get2(rs, lane * 2 + k * 128, G_GU + (fb & ~3) * 128);
    }
    if (t == TD && wave == 0) {
      gather<128, 0>(rs, gg, lane * 2, [&](int) { return G_GU + (fb & ~3) * 128; }, tlo, thi);
    float hv[2];
#pragma unroll
    for (int c = 0; c < 2; ++c) {
      float a0 = val(gg[0][2 * c + 1]), a1 = val(gg[1][2 * c + 1]), a2 = val(gg[2][2 * c + 1]), a3 = val(gg[3][2 * c + 1]);
      swap32(a0, a2); a0 += a2; swap32(a1, a3); a1 += a3;
      swap16(a0, a1); a0 += a1; a0 += dpp<0x128>(a0);
      const float gv = rbf(a0), uv = dpp<0x104>(gv);
      const float inner = 0.7978845608028654f * (gv + 0.044715f * gv * gv * gv);
      const float th = 1.0f - 2.0f / (__builtin_amdgcn_exp2f(inner * (2.0f * LOG2E)) + 1.0f);
      const float hh = rbf(rbf(0.5f * gv * (1.0f + th)) * uv);
      hv[c] = __int_as_float(__builtin_amdgcn_update_dpp(__float_as_int(hh), __float_as_int(hh), 0x114, 0xf, 0xa, false));
    }
    const float h0 = hv[0], h1 = hv[1];
    float d[8] = {0, 0, 0, 0, 0, 0, 0, 0};
#define DROW(k, hk)                                                                                    \
    { const float hb = hk;                                                                             \
      _Pragma("unroll") for (int c = 0; c < 4; ++c) { d[2 * c] += hb * bflo(wd[k][c]); d[2 * c + 1] += hb * bfhi(wd[k][c]); } }
    DROW(0, dpp<0x00>(h0)) DROW(1, dpp<0x00>(h1)) DROW(2, dpp<0x55>(h0)) DROW(3, dpp<0x55>(h1))
    DROW(4, dpp<0xAA>(h0)) DROW(5, dpp<0xAA>(h1)) DROW(6, dpp<0xFF>(h0)) DROW(7, dpp<0xFF>(h1))
#pragma unroll
    for (int k = 0; k < 4; ++k) { swap32(d[k], d[k + 4]); d[k] += d[k + 4]; }
#pragma unroll
    for (int k = 0; k < 2; ++k) { swap16(d[k], d[k + 2]); d[k] += d[k + 2]; }
    if (j < 28) put2(rs, G_D + fb * 128 + i * 8 + g * 2, d[0], d[1], tlo, thi, thi);
    }
    if (t == PREP) {
      pt0 = __builtin_amdgcn_raw_buffer_load_b32(rs, io * 8, 0, 16);
      pt1 = __builtin_amdgcn_raw_buffer_load_b32(rs, io * 8 + 256, 0, 16);
      pt2 = __builtin_amdgcn_raw_buffer_load_b32(rs, (b < 32 ? iy : io) * 8, 0, 16);
    }
    __builtin_amdgcn_sched_barrier(0);
  }
  l = xsum32(xsum16(l));
  *(f32x4*)&s_o[wave][lane * 4] = olo;
  *(f32x4*)&s_o[wave][256 + lane * 4] = ohi;
  if (lane < 4) s_ml[lane][wave] = f32x2{m * LOG2E, l};
  TS(4);
  wg_barrier();
  TS(5);
  __builtin_amdgcn_sched_barrier(0);
  {
    const f32x2 ml = s_ml[g][i & 7];
    float so[8];
#pragma unroll
    for (int w = 0; w < 8; ++w) so[w] = s_o[w][wave * 64 + 16 * (lane & 3) + 4 * g + e];
    const float M = oct_max(ml[0]);
    float f = __builtin_amdgcn_exp2f(ml[0] - M);
    const float L = oct_sum(f * ml[1]);
    f *= __builtin_amdgcn_rcpf(L);
    const unsigned lse = __float_as_uint(M + __builtin_amdgcn_logf(L));
    const float o = ((dpp<0x150>(f) * so[0] + dpp<0x151>(f) * so[1]) + (dpp<0x152>(f) * so[2] + dpp<0x153>(f) * so[3])) +
                    ((dpp<0x154>(f) * so[4] + dpp<0x155>(f) * so[5]) + (dpp<0x156>(f) * so[6] + dpp<0x157>(f) * so[7]));
    const float o1 = dpp<0xB1>(o);
    const unsigned sp = lse >> (2 * i);
    if (!(lane & 1)) put2(rs, G_O + b * 512 + g * 128 + wave * 16 + i, o, o1, tlo, thi | ((sp & 3) << 6), thi | ((sp & 12) << 4));
  }
  if (b < 32 && wave == GW) {
#pragma unroll
    for (int k = 0; k < 7; ++k) dg[k] = get2(rs, id, k * 4096);
  }
  TS(6);
  __builtin_amdgcn_sched_barrier(0);
  {
    asm volatile("s_waitcnt vmcnt(0)" ::: "memory");
    og[0] = og[1] = u32x4{~tlo, 0, 0, 0};
    gather<32, ON>(rs, og, io, [&](int) { return 0; }, tlo, thi);
    asm volatile("" :: "v"(pt0), "v"(pt1), "v"(pt2));
    TS(7);
    unsigned lb = (((og[0][1] >> 6) & 3) | ((og[0][3] >> 4) & 12)) << (4 * (i & 7));
    lb |= __builtin_amdgcn_update_dpp(0, lb, 0xB1, 0xf, 0xf, true);
    lb |= __builtin_amdgcn_update_dpp(0, lb, 0x4E, 0xf, 0xf, true);
    lb |= __builtin_amdgcn_update_dpp(0, lb, 0x141, 0xf, 0xf, true);
    const float lse = __uint_as_float(lb);
    const float mw = xmax32(xmax16(lse));
    const float f = __builtin_amdgcn_exp2f(lse - mw);
    float v0 = val(og[0][1]) * f, v1 = val(og[0][3]) * f, v2 = val(og[1][1]) * f, v3 = val(og[1][3]) * f;
    swap32(v0, v2); v0 += v2; swap32(v1, v3); v1 += v3;
    swap16(v0, v1); v0 += v1;
    s_p[wave][(lane >> 5) * 32 + 2 * i + (g & 1)] = v0;
    const float lw = xsum32(xsum16(f));
    if (lane == 0) s_wl[wave] = f32x2{mw, lw};
    wg_barrier();
    const f32x2 wl = s_wl[i & 7];
    const float pv = s_p[i & 7][q0 + (i >> 3) * 32];
    const float F = __builtin_amdgcn_exp2f(wl[0] - oct_max(wl[0]));
    const float oa = rbf(oct_sum(F * pv) * __builtin_amdgcn_rcpf(oct_sum(F * wl[1])));
    const float o0 = dpp<0x150>(oa), o1 = dpp<0x158>(oa);
    float y[8];
#pragma unroll
    for (int c = 0; c < 4; ++c) {
      y[2 * c] = o0 * bflo(wo0[c]) + o1 * bflo(wo1[c]);
      y[2 * c + 1] = o0 * bfhi(wo0[c]) + o1 * bfhi(wo1[c]);
    }
#pragma unroll
    for (int k = 0; k < 4; ++k) { swap32(y[k], y[k + 4]); y[k] += y[k + 4]; }
#pragma unroll
    for (int k = 0; k < 2; ++k) { swap16(y[k], y[k + 2]); y[k] += y[k + 2]; }
    *(f32x2*)&s_y[wave][i * 8 + g * 2] = f32x2{y[0], y[1]};
    wg_barrier();
    const f32x2 yy = *(const f32x2*)&s_y[lane >> 3][wave * 16 + (lane & 7) * 2];
    float ya = yy[0], yb = yy[1];
    ya = xsum32(xsum16(ya)); yb = xsum32(xsum16(yb));
    ya += dpp<0x128>(ya); yb += dpp<0x128>(yb);
    if (lane < 8) put2(rs, G_Y + b * 128 + wave * 16 + lane * 2, ya, yb, tlo, thi, thi);
  }
  TS(8);
  if (b < 32) {
    const unsigned xx = *(const unsigned*)(X + b * 32 + (lane & 7) * 4);
    if (wave == GW) {
      gather<0, 0>(rs, dg, id, [&](int k) { return k * 4096; }, tlo, thi);
      float d0 = 0, d1 = 0;
#pragma unroll
      for (int k = 0; k < 7; ++k) { d0 += val(dg[k][1]); d1 += val(dg[k][3]); }
      swap32(d0, d1); d0 += d1;
      d0 = xsum16(d0); d0 += dpp<0x128>(d0);
      d0 = rbf(d0);
      s_g[lane] = rbf(d0 / (1.0f + __builtin_amdgcn_exp2f(-d0 * LOG2E)));
    }
    yg[0] = u32x4{~tlo, 0, 0, 0};
    gather<0, 0>(rs, yg, iy, [&](int) { return 0; }, tlo, thi);
    float ya = val(yg[0][1]), yb = val(yg[0][3]);
    swap32(ya, yb); ya += yb;
    ya = xsum16(ya); ya += dpp<0x128>(ya);
    if (!(lane & 24)) s_f[wave][(lane >> 5) * 8 + (lane & 7)] = ya;
    wg_barrier();
    TS(9);
    if (wave == 0) {
      const int c = (lane >> 5) * 8 + (lane & 7), w = (lane >> 3) & 3;
      const float gate = s_g[lane];
      float y = s_f[w][c] + s_f[w + 4][c];
      y = xsum16(y); y += dpp<0x128>(y);
      float r0 = rbf(rbf(y) * gate), r1 = r0;
      swap32(r0, r1);
      if (lane < 8) *(unsigned*)(out + b * 16 + lane * 2) = pk_bf16(bflo(xx) + r0, bfhi(xx) + r1);
      if (b == 0 && lane == 0) __hip_atomic_store((gu64*)ws + G_E, T, __ATOMIC_RELAXED, __HIP_MEMORY_SCOPE_AGENT);
    }
  } else { TS(9); }
  TS(10);
  asm volatile(".Lcp_e:");
}

extern "C" void kernel_launch(void* const* d_in, const int* in_sizes, int n_in, void* d_out, int out_size, void* d_ws,
                              size_t ws_size, hipStream_t stream) {
  fused_decode<<<256, 512, 0, stream>>>((const char*)d_in[0], (const char*)d_in[1], (const char*)d_in[2], (const char*)d_in[3],
                                        (const char*)d_in[4], (const char*)d_in[6], (const char*)d_in[7], (const char*)d_in[8],
                                        (unsigned short*)d_out, (char*)d_ws);
}
```
